```python
import torch

def _softmax(x, axis=-1):
    return torch.softmax(x, dim=axis)
def _gelu(x):
    return torch.nn.functional.gelu(x, approximate="tanh")
def _silu(x):
    return torch.nn.functional.silu(x)
def _sigmoid(x):
    return torch.sigmoid(x)
def _layernorm(x, axis=-1, eps=1e-5):
    m = x.mean(dim=axis, keepdim=True)
    v = x.var(dim=axis, keepdim=True, unbiased=False)
    return (x - m) / torch.sqrt(v + eps)
def _scatter_add(tgt, idx, src, axis=0):
    return tgt.clone().index_add_(axis, idx, src)
def _segment_sum(src, seg, n):
    out = torch.zeros((n,) + src.shape[1:], dtype=src.dtype)
    return out.index_add_(0, seg, src)
def _pad(x, pw):
    flat = tuple(v for pair in reversed(pw) for v in pair)
    return torch.nn.functional.pad(x, flat)

kernel_name = 'sequential_preLN__sliding_block_cached__glu_gated_glu__decode'

def setup_inputs():

    _w = (lambda a: (lambda t: t.to(torch.bfloat16) if t.is_floating_point() else t)(torch.as_tensor(a)))
    def _A_setup():
        B, Sq, D = 8, 1, 512
        Hq, Hkv, Sk, Dh = 32, 8, 4096, 128
        assert Hq % Hkv == 0, (Hq, Hkv)
        return {
            "x": _w((lambda *s: torch.randn(*s, dtype=torch.bfloat16) * 0.05)(B, Sq, D)),
            "k_cache": _w((lambda *s: torch.randn(*s, dtype=torch.bfloat16) * 3.2)(B, Hkv, Sk, Dh)),
            "v_cache": _w((lambda *s: torch.randn(*s, dtype=torch.bfloat16) * 0.4)(B, Hkv, Sk, Dh)),
            "wq": _w((lambda *s: torch.randn(*s, dtype=torch.bfloat16) * 3.2)(D, Hq * Dh) * (D ** -0.5)),
            "wo": _w((lambda *s: torch.randn(*s, dtype=torch.bfloat16) * 0.4)(Hq * Dh, D) * ((Hq * Dh) ** -0.5)),
        }
    _A_in = _A_setup()

    def _B_setup():
        B, S, D, F = 8, 1, 512, 1792
        return {
            "x": _w((lambda *s: torch.randn(*s, dtype=torch.bfloat16) * 0.05)(B, S, D)),
            "w_gate": _w((lambda *s: torch.randn(*s, dtype=torch.bfloat16) * 0.1)(D, F)),
            "w_up": _w((lambda *s: torch.randn(*s, dtype=torch.bfloat16) * 0.05)(D, F)),
            "w_down": _w((lambda *s: torch.randn(*s, dtype=torch.bfloat16) * 0.05)(F, D)),
        }
    _B_in = _B_setup()

    return {
        "A_x": _A_in["x"],
        "A_k_cache": _A_in["k_cache"],
        "A_v_cache": _A_in["v_cache"],
        "A_wq": _A_in["wq"],
        "A_wo": _A_in["wo"],
        "B_x": _B_in["x"],
        "B_w_gate": _B_in["w_gate"],
        "B_w_up": _B_in["w_up"],
        "B_w_down": _B_in["w_down"],
    }

def reference(A_x, A_k_cache, A_v_cache, A_wq, A_wo, B_x, B_w_gate, B_w_up, B_w_down):
    def _A_ref(x, k_cache, v_cache, wq, wo):
        B, Sq, D = x.shape
        Hq, Dh = 32, 128
        Hkv, Sk = k_cache.shape[1], k_cache.shape[2]
        W = min(4096, max(256, Sk // 4))
        rep = Hq // Hkv
        q = (lambda x, *d: x.permute(*d))((x @ wq).reshape(B, Sq, Hq, Dh), 0, 2, 1, 3)
        k = (lambda x, n, axis: torch.repeat_interleave(x, n, dim=axis))(k_cache[:, :, Sk - W:, :], rep, axis=1)
        v = (lambda x, n, axis: torch.repeat_interleave(x, n, dim=axis))(v_cache[:, :, Sk - W:, :], rep, axis=1)
        scores = torch.einsum("bhqd,bhkd->bhqk", q, k) * (Dh ** -0.5)
        p = _softmax(scores, axis=-1)
        o = torch.einsum("bhqk,bhkd->bhqd", p, v)
        return (lambda x, *d: x.permute(*d))(o, 0, 2, 1, 3).reshape(B, Sq, Hq * Dh) @ wo

    def _B_ref(x, w_gate, w_up, w_down):
        g = torch.einsum("bsd,df->bsf", x, w_gate)
        u = torch.einsum("bsd,df->bsf", x, w_up)
        h = _sigmoid(g) * u
        return torch.einsum("bsf,fd->bsd", h, w_down)

    x = A_x
    h = x + _A_ref(_layernorm(x, axis=-1), A_k_cache, A_v_cache, A_wq, A_wo)
    h2 = h + _B_ref(_layernorm(h, axis=-1), B_w_gate, B_w_up, B_w_down)
    return h2
```

```cpp
#include <hip/hip_runtime.h>
#include <hip/hip_bf16.h>
#include <stdint.h>
#define MIMIC 0
typedef unsigned short u16;
__device__ __forceinline__ float bf2f(u16 b){ return __uint_as_float(((unsigned)b)<<16); }
__device__ __forceinline__ u16 f2bf(float f){ unsigned u=__float_as_uint(f); u += 0x7FFFu + ((u>>16)&1u); return (u16)(u>>16); }
__device__ __forceinline__ float rb(float f){
  return f;
}
constexpr int B=8, D=512, HQ=32, HKV=8, SK=4096, DH=128, W=1024, F=1792;

__device__ void layernorm_row(const float* xin, float* xn, float* red){
  int t=threadIdx.x;
  float a=xin[t], b=xin[t+256];
  red[t]=a+b; __syncthreads();
  for(int s=128;s>0;s>>=1){ if(t<s) red[t]+=red[t+s]; __syncthreads(); }
  float mean=red[0]/D; __syncthreads();
  float da=a-mean, db=b-mean;
  red[t]=da*da+db*db; __syncthreads();
  for(int s=128;s>0;s>>=1){ if(t<s) red[t]+=red[t+s]; __syncthreads(); }
  float var=red[0]/D; __syncthreads();
  float m=rb(mean), v=rb(var);
  float den=rb(sqrtf(rb(v+1e-5f)));
  xn[t]=rb(rb(a-m)/den); xn[t+256]=rb(rb(b-m)/den);
  __syncthreads();
}
__global__ void k_q(const u16* x, const u16* wq, float* q){
  __shared__ float xin[D], xn[D], red[256];
  int b=blockIdx.y, t=threadIdx.x;
  xin[t]=bf2f(x[b*D+t]); xin[t+256]=bf2f(x[b*D+t+256]); __syncthreads();
  layernorm_row(xin,xn,red);
  int n=blockIdx.x*256+t;
  float acc=0;
  for(int k=0;k<D;k++) acc+=xn[k]*bf2f(wq[(size_t)k*4096+n]);
  q[b*4096+n]=rb(acc);
}
__global__ void k_attn(const float* q, const u16* kc, const u16* vc, float* o){
  __shared__ float s[W], red[256], qs[DH];
  int h=blockIdx.x, b=blockIdx.y, t=threadIdx.x, hk=h/4;
  if(t<DH) qs[t]=q[b*4096+h*DH+t];
  __syncthreads();
  const u16* kb=kc+((size_t)(b*HKV+hk)*SK+(SK-W))*DH;
  const u16* vb=vc+((size_t)(b*HKV+hk)*SK+(SK-W))*DH;
  float mx=-INFINITY;
  for(int j=t;j<W;j+=256){
    float acc=0;
    for(int d=0;d<DH;d++) acc+=qs[d]*bf2f(kb[(size_t)j*DH+d]);
    float sc=rb(rb(acc)*0.08838834764831845f);
    s[j]=sc; mx=fmaxf(mx,sc);
  }
  red[t]=mx; __syncthreads();
  for(int st=128;st>0;st>>=1){ if(t<st) red[t]=fmaxf(red[t],red[t+st]); __syncthreads(); }
  mx=red[0]; __syncthreads();
  float sum=0;
  for(int j=t;j<W;j+=256){ float e=expf(s[j]-mx); s[j]=e; sum+=e; }
  red[t]=sum; __syncthreads();
  for(int st=128;st>0;st>>=1){ if(t<st) red[t]+=red[t+st]; __syncthreads(); }
  sum=red[0]; __syncthreads();
  for(int j=t;j<W;j+=256) s[j]=rb(s[j]/sum);
  __syncthreads();
  if(t<DH){
    float acc=0;
    for(int j=0;j<W;j++) acc+=s[j]*bf2f(vb[(size_t)j*DH+t]);
    o[b*4096+h*DH+t]=rb(acc);
  }
}
__global__ void k_o(const float* o, const u16* wo, const u16* x, float* hbuf){
  __shared__ float os[4096];
  int b=blockIdx.y, t=threadIdx.x;
  for(int i=t;i<4096;i+=256) os[i]=o[b*4096+i];
  __syncthreads();
  int n=blockIdx.x*256+t;
  float acc=0;
  for(int k=0;k<4096;k++) acc+=os[k]*bf2f(wo[(size_t)k*D+n]);
  hbuf[b*D+n]=rb(bf2f(x[b*D+n])+rb(acc));
}
__global__ void k_gu(const float* hbuf, const u16* wg, const u16* wu, float* hh){
  __shared__ float xin[D], xn[D], red[256];
  int b=blockIdx.y, t=threadIdx.x;
  xin[t]=hbuf[b*D+t]; xin[t+256]=hbuf[b*D+t+256]; __syncthreads();
  layernorm_row(xin,xn,red);
  int n=blockIdx.x*256+t;
  float g=0,u=0;
  for(int k=0;k<D;k++){ g+=xn[k]*bf2f(wg[(size_t)k*F+n]); u+=xn[k]*bf2f(wu[(size_t)k*F+n]); }
  g=rb(g); u=rb(u);
  float sg=rb(1.f/(1.f+expf(-g)));
  hh[b*F+n]=rb(sg*u);
}
__global__ void k_down(const float* hh, const u16* wd, const float* hbuf, u16* out){
  __shared__ float hs[F];
  int b=blockIdx.y, t=threadIdx.x;
  for(int i=t;i<F;i+=256) hs[i]=hh[b*F+i];
  __syncthreads();
  int n=blockIdx.x*256+t;
  float acc=0;
  for(int k=0;k<F;k++) acc+=hs[k]*bf2f(wd[(size_t)k*D+n]);
  out[b*D+n]=f2bf(hbuf[b*D+n]+rb(acc));
}
extern "C" void kernel_launch(void* const* d_in, const int* in_sizes, int n_in,
                              void* d_out, int out_size, void* d_ws, size_t ws_size,
                              hipStream_t stream) {
  const u16* x=(const u16*)d_in[0]; const u16* kc=(const u16*)d_in[1]; const u16* vc=(const u16*)d_in[2];
  const u16* wq=(const u16*)d_in[3]; const u16* wo=(const u16*)d_in[4];
  const u16* wg=(const u16*)d_in[6]; const u16* wu=(const u16*)d_in[7]; const u16* wd=(const u16*)d_in[8];
  float* q=(float*)d_ws; float* o=q+B*4096; float* hb=o+B*4096; float* hh=hb+B*D;
  k_q<<<dim3(16,B),256,0,stream>>>(x,wq,q);
  k_attn<<<dim3(HQ,B),256,0,stream>>>(q,kc,vc,o);
  k_o<<<dim3(2,B),256,0,stream>>>(o,wo,x,hb);
  k_gu<<<dim3(7,B),256,0,stream>>>(hb,wg,wu,hh);
  k_down<<<dim3(2,B),256,0,stream>>>(hh,wd,hb,(u16*)d_out);
}
```

```cpp
#include <hip/hip_runtime.h>
#include <hip/hip_bf16.h>
#include <stdint.h>

#define STAMPS 0
#define NT_KV 0
#define NT_W 0
#define KV_WIN 12
#define Q_DELAY 8

#define Q_DLY 0
#define Q_SLP 2
#define Q_MASK 0
#define CO_DLY 0
#define CO_SLP 0
#define CO_MASK 0
#define R_DLY 0
#define R_SLP 0
#define R_MASK 0
#define H_DLY 0
#define H_SLP 0
#define H_MASK 0
#define R2_DLY 0
#define R2_SLP 1
#define R2_MASK 0

namespace fk {
#define LAS __attribute__((address_space(3)))
typedef unsigned v4u __attribute__((ext_vector_type(4)));
typedef unsigned v2u __attribute__((ext_vector_type(2)));
typedef float f32x4 __attribute__((ext_vector_type(4)));
typedef float f32x2 __attribute__((ext_vector_type(2)));
typedef short s16x4 __attribute__((ext_vector_type(4)));
typedef __bf16 bf16x8 __attribute__((ext_vector_type(8)));
typedef __bf16 bf16x2 __attribute__((ext_vector_type(2)));
typedef unsigned long long u64;
typedef __amdgpu_buffer_rsrc_t rsrc_t;

constexpr int NB = 8, DM = 512, NHKV = 8, SK = 4096, WIN = 1024, DH = 128, FF = 1792;
constexpr int NMLP = 56;
constexpr float SCALE = 0.08838834764831845f;
constexpr float LOG2E = 1.4426950408889634f;
constexpr unsigned SPIN_CAP = 1u << 15;

constexpr unsigned WS_E = 0;
constexpr unsigned WS_QG = 4096;
constexpr unsigned WS_CO = WS_QG + 8 * 8 * 4 * 512 * 8;
constexpr unsigned CO_BLK = 2176;
constexpr unsigned WS_RP = WS_CO + 256 * CO_BLK;
constexpr unsigned WS_HG = WS_RP + 32 * 8 * 512 * 8;
constexpr unsigned WS_R2 = WS_HG + 8 * 256 * 8;
constexpr unsigned WS_END = WS_R2 + NMLP * 16384;
constexpr unsigned WS_ST = 6u << 20;
constexpr unsigned WS_BYTES = STAMPS ? (8u << 20) : WS_END;

constexpr unsigned L_K = 0, L_V = 65536, L_WQ = 131072, L_XN = 147456, XN_S = 1056;
constexpr unsigned L_Q = L_XN + 8 * XN_S, Q_S = 320;
constexpr unsigned L_P = L_Q + 4 * Q_S, P_S = 576;
constexpr unsigned L_RED = L_P + 4 * P_S;
constexpr unsigned L_ST = L_RED + 2048;
constexpr unsigned L_CT = L_ST + 128;
constexpr unsigned LDS_BYTES = L_CT + 256;
static_assert(LDS_BYTES <= 163840, "LDS");
constexpr unsigned L_O = L_WQ, O_S = 288;
constexpr unsigned L_RR = L_WQ + 4096, L_RR2 = L_WQ + 6144;
constexpr unsigned L_HH = L_WQ + 8192, HH_S = 96;
constexpr unsigned L_G = L_K, L_U = L_K + 32768;
enum { F_XW = 0, F_Q = 1, F_MAX = 2, F_P = 3, F_PV = 4, F_K = 5, F_VI = 6, F_V = 7  , F_TAG = 11, F_KI = 12, F_N = 13 };

struct Params {
  const void* x; const void* kc; const void* vc; const void* wq; const void* wo;
  const void* wg; const void* wu; const void* wd; void* out; void* ws;
};

__device__ __forceinline__ rsrc_t mk_rsrc(const void* p, unsigned bytes) {
  return __builtin_amdgcn_make_buffer_rsrc((void*)p, 0, bytes, 0x00020000);
}
__device__ __forceinline__ unsigned pk(float a, float b) { bf16x2 v = {(__bf16)a, (__bf16)b}; return __builtin_bit_cast(unsigned, v); }
__device__ __forceinline__ float rb(float a) { return (float)(__bf16)a; }
__device__ __forceinline__ float lo16(unsigned u) { return __uint_as_float(u << 16); }
__device__ __forceinline__ float hi16(unsigned u) { return __uint_as_float(u & 0xffff0000u); }
__device__ __forceinline__ f32x4 mfma(v4u a, v4u b, f32x4 c) {
  return __builtin_amdgcn_mfma_f32_16x16x32_bf16(__builtin_bit_cast(bf16x8, a), __builtin_bit_cast(bf16x8, b), c, 0, 0, 0);
}
template <int CTRL> __device__ __forceinline__ float dpp(float x) {
  return __builtin_bit_cast(float, __builtin_amdgcn_mov_dpp(__builtin_bit_cast(int, x), CTRL, 0xf, 0xf, true));
}
constexpr int XOR1 = 0xB1, XOR2 = 0x4E, HMIR = 0x141, MIR = 0x140, ROR4 = 0x124, ROR8 = 0x128;
__device__ __forceinline__ float x16_sum(float x) {
  auto s = __builtin_amdgcn_permlane16_swap(__float_as_uint(x), __float_as_uint(x), false, false);
  return __uint_as_float(s[0]) + __uint_as_float(s[1]);
}
__device__ __forceinline__ float x32_sum(float x) {
  auto s = __builtin_amdgcn_permlane32_swap(__float_as_uint(x), __float_as_uint(x), false, false);
  return __uint_as_float(s[0]) + __uint_as_float(s[1]);
}
__device__ __forceinline__ float x16_max(float x) {
  auto s = __builtin_amdgcn_permlane16_swap(__float_as_uint(x), __float_as_uint(x), false, false);
  return fmaxf(__uint_as_float(s[0]), __uint_as_float(s[1]));
}
__device__ __forceinline__ float x32_max(float x) {
  auto s = __builtin_amdgcn_permlane32_swap(__float_as_uint(x), __float_as_uint(x), false, false);
  return fmaxf(__uint_as_float(s[0]), __uint_as_float(s[1]));
}
__device__ __forceinline__ float wave_sum(float x) {
  x += dpp<XOR1>(x); x += dpp<XOR2>(x); x += dpp<HMIR>(x); x += dpp<MIR>(x);
  return x32_sum(x16_sum(x));
}
__device__ __forceinline__ void layernorm8(float (&v)[8]) {
  const float pv = __uint_as_float(__builtin_amdgcn_readfirstlane(__float_as_uint(v[0])));
  float d[8];
#pragma unroll
  for (int i = 0; i < 8; ++i) d[i] = v[i] - pv;
  float s = ((d[0] + d[1]) + (d[2] + d[3])) + ((d[4] + d[5]) + (d[6] + d[7]));
  float q = fmaf(d[0], d[0], d[1] * d[1]) + fmaf(d[2], d[2], d[3] * d[3]) + (fmaf(d[4], d[4], d[5] * d[5]) + fmaf(d[6], d[6], d[7] * d[7]));
  s += dpp<XOR1>(s); q += dpp<XOR1>(q);
  s += dpp<XOR2>(s); q += dpp<XOR2>(q);
  s += dpp<HMIR>(s); q += dpp<HMIR>(q);
  s += dpp<MIR>(s);  q += dpp<MIR>(q);
  s = x16_sum(s); q = x16_sum(q);
  s = x32_sum(s) * (1.0f / 512.0f); q = x32_sum(q) * (1.0f / 512.0f);
  const float var = fmaxf(fmaf(-s, s, q), 0.f);
  const float mb = rb(pv + s);
  const float den = rb(__builtin_amdgcn_sqrtf(rb(rb(var) + 1e-5f)));
  const float inv = __builtin_amdgcn_rcpf(den);
#pragma unroll
  for (int i = 0; i < 8; ++i) v[i] = rb(v[i] - mb) * inv;
}

template <int IMM, int NT>
__device__ __forceinline__ void dma16(unsigned m0v, unsigned voff, rsrc_t srd, unsigned soff) {
  if constexpr (NT)
    asm volatile("s_mov_b32 m0, %0\n\ts_nop 0\n\tbuffer_load_dwordx4 %1, %2, %3 offen offset:%4 nt lds"
                 :: "s"(m0v), "v"(voff), "s"(srd), "s"(soff), "i"(IMM) : "memory");
  else
    asm volatile("s_mov_b32 m0, %0\n\ts_nop 0\n\tbuffer_load_dwordx4 %1, %2, %3 offen offset:%4 lds"
                 :: "s"(m0v), "v"(voff), "s"(srd), "s"(soff), "i"(IMM) : "memory");
}
#define VMWAIT(n) asm volatile("s_waitcnt vmcnt(" #n ")" ::: "memory")
template <int N> __device__ __forceinline__ void vmwait() { asm volatile("s_waitcnt vmcnt(%0)" :: "i"(N) : "memory"); }
#define LGKM0() asm volatile("s_waitcnt lgkmcnt(0)" ::: "memory")
__device__ __forceinline__ void wg_barrier() { LGKM0(); __builtin_amdgcn_s_barrier(); asm volatile("" ::: "memory"); }

__device__ __forceinline__ void flag_set(unsigned ldsb, int grp, int slot) {
  const unsigned a = ldsb + L_CT + grp * 16 + slot * 4, one = 1u;
  asm volatile("s_waitcnt lgkmcnt(0)\n\ts_mov_b64 exec, 1\n\tds_write_b32 %0, %1\n\ts_mov_b64 exec, -1" :: "v"(a), "v"(one) : "memory");
}
template <int N>
__device__ __forceinline__ void flag_wait(LAS unsigned char* L, int grp) {
  for (unsigned spins = 0; spins < (1u << 22); ++spins) {
    asm volatile("" ::: "memory");
    const v4u v = *(volatile LAS v4u*)(L + L_CT + grp * 16);
    const unsigned all = N == 4 ? (v.x & v.y & v.z & v.w) : (v.x & v.y);
    if (__builtin_amdgcn_readfirstlane(all)) break;
  }
  asm volatile("" ::: "memory");
}
#define PIN(x) asm volatile("" : "+v"(x))
__device__ __forceinline__ v4u ld128(LAS unsigned char* L, unsigned off) { return *(LAS v4u*)(L + off); }
__device__ __forceinline__ v4u tr_frag(LAS unsigned char* L, unsigned off, unsigned second) {
  const s16x4 a = __builtin_amdgcn_ds_read_tr16_b64_v4i16((LAS s16x4*)(L + off));
  const s16x4 b = __builtin_amdgcn_ds_read_tr16_b64_v4i16((LAS s16x4*)(L + off + second));
  const v2u x = __builtin_bit_cast(v2u, a), y = __builtin_bit_cast(v2u, b);
  return v4u{x.x, x.y, y.x, y.y};
}
__device__ __forceinline__ v4u gld128(rsrc_t rs, unsigned off) { return __builtin_amdgcn_raw_buffer_load_b128(rs, off, 0, 16); }
__device__ __forceinline__ v2u gld64(rsrc_t rs, unsigned off) { return __builtin_amdgcn_raw_buffer_load_b64(rs, off, 0, 16); }
__device__ __forceinline__ void gst128(rsrc_t rs, unsigned off, v4u v) { __builtin_amdgcn_raw_buffer_store_b128(v, rs, off, 0, 16); }
__device__ __forceinline__ void gst64(rsrc_t rs, unsigned off, v2u v) { __builtin_amdgcn_raw_buffer_store_b64(v, rs, off, 0, 16); }

template <int N, int DLY, int SLP, int MASK>
__device__ __forceinline__ unsigned gather(rsrc_t ws, unsigned off, unsigned stride, unsigned tag, v4u (&v)[N]) {
  if (DLY) __builtin_amdgcn_s_sleep(DLY);
  unsigned spins = 0;
  if (MASK) {
    bool need[N];
#pragma unroll
    for (int k = 0; k < N; ++k) need[k] = true;
    for (;; ++spins) {
      asm volatile("" ::: "memory");
#pragma unroll
      for (int k = 0; k < N; ++k) if (need[k]) v[k] = gld128(ws, off + k * stride);
      bool any = false;
#pragma unroll
      for (int k = 0; k < N; ++k) { if (need[k]) need[k] = !__all(v[k].y == tag && v[k].w == tag); any = any || need[k]; }
      if (!any || spins >= SPIN_CAP) break;
      if (SLP) __builtin_amdgcn_s_sleep(SLP);
    }
  } else {
    for (;; ++spins) {
      asm volatile("" ::: "memory");
#pragma unroll
      for (int k = 0; k < N; ++k) v[k] = gld128(ws, off + k * stride);
      bool ok = true;
#pragma unroll
      for (int k = 0; k < N; ++k) ok = ok && v[k].y == tag && v[k].w == tag;
      if (__all(ok) || spins >= SPIN_CAP) break;
      if (SLP) __builtin_amdgcn_s_sleep(SLP);
    }
  }
  return spins + 1;
}

#define STAMP(i) do {} while (0)
#define COUNT(i, v) do {} while (0)

__global__ void __launch_bounds__(512, 2) fused_decode(Params p) {
  __shared__ __attribute__((aligned(16))) unsigned char lds_raw[LDS_BYTES];
  LAS unsigned char* L = (LAS unsigned char*)lds_raw;
  const unsigned ldsb = (unsigned)(size_t)L;
  STAMP(0);
  asm volatile("" :: "s"(p.x), "s"(p.kc), "s"(p.vc), "s"(p.wq), "s"(p.wo), "s"(p.wg), "s"(p.wu), "s"(p.wd), "s"(p.out), "s"(p.ws));
  const int tid = threadIdx.x, lane = tid & 63;
  const int w = __builtin_amdgcn_readfirstlane(tid >> 6);
  const int c = blockIdx.x, hkv = c & 7, j = c >> 3, b = j >> 2, s = j & 3;
  const int g = lane >> 4, i16 = lane & 15;
  const rsrc_t ws = mk_rsrc(p.ws, WS_BYTES);
  unsigned tag = 0;
  const bool roleA = j < 16;
  const int hi4 = hkv >> 2;
  const bool roleB = j >= 16 && j - 16 < 8 - 2 * hi4;
  const int m = (8 - 2 * hi4) * hkv + 8 * hi4 + (j - 16);
  const int hl = j >> 2, cb128 = j & 3, head = 4 * hkv + hl;
  const unsigned coblk = WS_CO + (unsigned)(hkv * 32 + j) * CO_BLK;
  const int rot = (j + 3 * hkv) & 7;

  if (w >= 6) {
    if (!(roleA || roleB)) return;
    const int l = w - 6;
    __builtin_amdgcn_s_barrier();
    asm volatile("" ::: "memory");
    if (roleA) {
      const rsrc_t ro = mk_rsrc((const char*)p.wo + (size_t)head * 128 * 1024 + cb128 * 256, 128 * 1024 - cb128 * 256);
      unsigned vo[2];
#pragma unroll
      for (int r = 0; r < 2; ++r) vo[r] = g * 1024 + ((i16 ^ (((4 * r + g) & 7) << 1)) * 16);
      flag_wait<2>(L, F_VI);
      flag_wait<4>(L, F_MAX);
      STAMP(1);
#pragma unroll
      for (int q = 0; q < 16; ++q) dma16<0, NT_W>(ldsb + L_K + (64 * l + 4 * q) * 256, vo[q & 1], ro, (64 * l + 4 * q) * 1024);
      STAMP(2);
      VMWAIT(0);
    } else {
      const rsrc_t rg = mk_rsrc((const char*)(l == 0 ? p.wg : p.wu) + m * 64, 512 * FF * 2 - m * 64);
      const unsigned vg = (lane >> 2) * (FF * 2) + (((lane & 3) ^ (2 * ((lane >> 4) & 1) + l)) * 16);
      const rsrc_t rd = mk_rsrc((const char*)p.wd + (size_t)m * 32 * 1024, 32 * 1024);
      flag_wait<2>(L, F_VI);
      flag_wait<4>(L, F_MAX);
      STAMP(1);
#pragma unroll
      for (int q = 0; q < 32; ++q) {
        const unsigned qq = (q + 5 * m) & 31;
        dma16<0, NT_W>(ldsb + L_G + l * 32768 + qq * 1024, vg, rg, qq * 16 * (FF * 2));
      }
      STAMP(2);
      flag_wait<4>(L, F_PV);
#pragma unroll
      for (int q = 0; q < 16; ++q) dma16<0, NT_W>(ldsb + L_V + (16 * l + q) * 1024, (unsigned)(lane ^ ((q & 7) << 1)) * 16, rd, (16 * l + q) * 1024);
      STAMP(3);
      VMWAIT(0);
    }
    STAMP(4);
  } else if (w >= 4) {
    const int l = w - 4;
    const size_t win = ((size_t)(b * NHKV + hkv) * SK + (SK - WIN) + 256 * s) * (DH * 2);
    const rsrc_t rk = mk_rsrc((const char*)p.kc + win, 65536), rv = mk_rsrc((const char*)p.vc + win, 65536);
    unsigned vk[4], vv[2];
#pragma unroll
    for (int r = 0; r < 4; ++r) vk[r] = g * 256 + ((i16 ^ (4 * r + g)) * 16);
#pragma unroll
    for (int r = 0; r < 2; ++r) vv[r] = g * 256 + ((i16 ^ (((4 * r + g) & 7) << 1)) * 16);
    asm volatile("s_nop 4" ::: "memory");
#define WIN_WAIT() vmwait<KV_WIN - 1>()
#define GOFF(a) ((unsigned)(32 * (((a) + rot) & 7) + 16 * l) * 256)
#define K_ONE(a, r) dma16<1024 * (r), NT_KV>(ldsb + L_K + GOFF(a), vk[r], rk, GOFF(a))
#define V_ONE(a, r) dma16<1024 * (r), NT_KV>(ldsb + L_V + GOFF(a), vv[(r) & 1], rv, GOFF(a))
#define K_GROUP(a) { WIN_WAIT(); K_ONE(a, 0); WIN_WAIT(); K_ONE(a, 1); WIN_WAIT(); K_ONE(a, 2); WIN_WAIT(); K_ONE(a, 3); }
#define V_GROUP(a) { WIN_WAIT(); V_ONE(a, 0); WIN_WAIT(); V_ONE(a, 1); WIN_WAIT(); V_ONE(a, 2); WIN_WAIT(); V_ONE(a, 3); }
    K_ONE(0, 0); K_ONE(0, 1); K_ONE(0, 2); K_ONE(0, 3);
    __builtin_amdgcn_s_barrier();
    asm volatile("" ::: "memory");
    VMWAIT(3);
    K_GROUP(1) K_GROUP(2) K_GROUP(3) K_GROUP(4) K_GROUP(5) K_GROUP(6) K_GROUP(7)
    flag_set(ldsb, F_KI, l);
    STAMP(1);
    V_GROUP(0) V_GROUP(1) V_GROUP(2)
    vmwait<(KV_WIN < 12 ? KV_WIN : 12)>();
    flag_set(ldsb, F_K, l);
    STAMP(2);
    V_GROUP(3) V_GROUP(4) V_GROUP(5) V_GROUP(6) V_GROUP(7)
    flag_set(ldsb, F_VI, l);
    STAMP(3);
    if (KV_WIN > 24) { VMWAIT(24); }
    flag_set(ldsb, F_V + 0, l);
    if (KV_WIN > 16) { VMWAIT(16); }
    flag_set(ldsb, F_V + 1, l);
    VMWAIT(8);  flag_set(ldsb, F_V + 2, l);
    VMWAIT(0);  flag_set(ldsb, F_V + 3, l);
    STAMP(4);
  } else {
    const int qcb = j & 7, kq = j >> 3;
    {
      const rsrc_t rx = mk_rsrc(p.x, NB * DM * 2);
      const int n0 = hkv * 512 + 64 * qcb;
      const rsrc_t rq = mk_rsrc((const char*)p.wq + (size_t)kq * 128 * 8192 + n0 * 2, 128 * 8192 - n0 * 2);
      const unsigned vq = (lane >> 3) * 8192 + (((lane & 7) ^ (((lane >> 4) & 3) << 1)) * 16);
      asm volatile("s_nop 4\n\tbuffer_load_dword %0, off, %1, 0 sc1" : "=&v"(tag) : "s"(ws) : "memory");
      dma16<0, 0>(ldsb + L_XN + (2 * w) * XN_S, lane * 16, rx, (2 * w) * 1024);
      dma16<0, 0>(ldsb + L_XN + (2 * w + 1) * XN_S, lane * 16, rx, (2 * w + 1) * 1024);
#pragma unroll
      for (int q = 0; q < 4; ++q) dma16<0, NT_W>(ldsb + L_WQ + (32 * w + 8 * q) * 128, vq, rq, (32 * w + 8 * q) * 8192);
    }
    if (w == 0 && lane < 4 * F_N) ((LAS unsigned*)(L + L_CT))[lane] = 0u;
    wg_barrier();
    asm volatile("s_waitcnt vmcnt(4)" : "+v"(tag) :: "memory");
    STAMP(1);
    tag += 1u;
    if (w == 0 && lane == 0) ((LAS unsigned*)(L + L_CT))[4 * F_TAG] = tag;
    {
      const v4u raw0 = ld128(L, L_XN + (2 * w) * XN_S + lane * 16), raw1 = ld128(L, L_XN + (2 * w + 1) * XN_S + lane * 16);
      float v0[8] = {lo16(raw0.x), hi16(raw0.x), lo16(raw0.y), hi16(raw0.y), lo16(raw0.z), hi16(raw0.z), lo16(raw0.w), hi16(raw0.w)};
      float v1[8] = {lo16(raw1.x), hi16(raw1.x), lo16(raw1.y), hi16(raw1.y), lo16(raw1.z), hi16(raw1.z), lo16(raw1.w), hi16(raw1.w)};
      layernorm8(v0); layernorm8(v1);
      const int t = lane >> 2, q = lane & 3;
      const unsigned base = L_XN + (2 * w) * XN_S + (32 * t + 16 * (q & 1) + 4 * (q >> 1)) * 2;
      asm volatile("" ::: "memory");
      *(LAS v2u*)(L + base) = v2u{pk(v0[0], v0[1]), pk(v0[2], v0[3])};
      *(LAS v2u*)(L + base + 16) = v2u{pk(v0[4], v0[5]), pk(v0[6], v0[7])};
      *(LAS v2u*)(L + base + XN_S) = v2u{pk(v1[0], v1[1]), pk(v1[2], v1[3])};
      *(LAS v2u*)(L + base + XN_S + 16) = v2u{pk(v1[4], v1[5]), pk(v1[6], v1[7])};
    }
    VMWAIT(0);
    STAMP(2);
    flag_set(ldsb, F_XW, w);
    flag_wait<4>(L, F_XW);
    const int rr = 4 * g + (i16 >> 2), pp = i16 & 3;
    {
      const unsigned ab = L_WQ + rr * 128 + (((2 * w + (pp >> 1)) ^ (((rr >> 1) & 3) << 1)) * 16) + 8 * (pp & 1);
      const unsigned bb = L_XN + (i16 & 7) * XN_S + (128 * kq + 8 * g) * 2;
      v4u a[4], bq[4];
#pragma unroll
      for (int t = 0; t < 4; ++t) { a[t] = tr_frag(L, ab + t * 4096, 2048); bq[t] = ld128(L, bb + t * 64); }
      f32x4 acc = {0.f, 0.f, 0.f, 0.f};
#pragma unroll
      for (int t = 0; t < 4; ++t) acc = mfma(bq[t], a[t], acc);
      const int b1 = 4 * (g & 1) + (g >> 1);
      const unsigned qa = WS_QG + (unsigned)(((hkv * 8 + b1) * 4 + kq) * 512 + 64 * qcb + 16 * w + i16) * 8;
      gst64(ws, qa, v2u{__float_as_uint(g < 2 ? acc[0] : acc[1]), tag});
      gst64(ws, qa + 2 * 4 * 512 * 8, v2u{__float_as_uint(g < 2 ? acc[2] : acc[3]), tag});
    }
    STAMP(3);
    {
      const unsigned off = WS_QG + (unsigned)((hkv * 8 + b) * 4 * 512 + 128 * w + 2 * lane) * 8;
      v4u v[4];
      flag_wait<2>(L, F_KI);
      { const unsigned n = gather<4, Q_DLY, Q_SLP, Q_MASK>(ws, off, 4096, tag, v); COUNT(16, n); }
      const float q0 = (__uint_as_float(v[0].x) + __uint_as_float(v[1].x)) + (__uint_as_float(v[2].x) + __uint_as_float(v[3].x));
      const float q1 = (__uint_as_float(v[0].z) + __uint_as_float(v[1].z)) + (__uint_as_float(v[2].z) + __uint_as_float(v[3].z));
      *(LAS unsigned*)(L + L_Q + w * Q_S + lane * 4) = pk(q0, q1);
    }
    flag_set(ldsb, F_Q, w);
    flag_wait<4>(L, F_Q);
    STAMP(4);
    float sc[4];
    const int hd = i16 & 3, dup = i16 >> 2;
    {
      v4u qf[4];
#pragma unroll
      for (int t = 0; t < 4; ++t) qf[t] = ld128(L, L_Q + hd * Q_S + (4 * t + g) * 16);
      flag_wait<2>(L, F_K);
      STAMP(5);
      v4u kf[16];
#pragma unroll
      for (int T = 0; T < 4; ++T)
#pragma unroll
        for (int t = 0; t < 4; ++t) kf[4 * T + t] = ld128(L, L_K + (64 * w + 16 * T + i16) * 256 + (((4 * t + g) ^ i16) * 16));
      __builtin_amdgcn_sched_barrier(0);
      f32x4 acc[4];
#pragma unroll
      for (int T = 0; T < 4; ++T) acc[T] = f32x4{0.f, 0.f, 0.f, 0.f};
#pragma unroll
      for (int t = 0; t < 4; ++t)
#pragma unroll
        for (int T = 0; T < 4; ++T) acc[T] = mfma(kf[4 * T + t], qf[t], acc[T]);
#pragma unroll
      for (int r = 0; r < 4; ++r) {
        const float a01 = (dup & 1) ? acc[1][r] : acc[0][r], a23 = (dup & 1) ? acc[3][r] : acc[2][r];
        sc[r] = rb(rb((dup & 2) ? a23 : a01) * SCALE);
      }
    }
    float mx, inv, lse;
    {
      float v = fmaxf(fmaxf(sc[0], sc[1]), fmaxf(sc[2], sc[3]));
      v = fmaxf(v, dpp<ROR4>(v)); v = fmaxf(v, dpp<ROR8>(v));
      v = x32_max(x16_max(v));
      if (lane < 4) ((LAS float*)(L + L_ST))[lane * 4 + w] = v;
      flag_set(ldsb, F_MAX, w);
      flag_wait<4>(L, F_MAX);
      const f32x4 m4 = *(LAS f32x4*)(L + L_ST + hd * 16);
      mx = fmaxf(fmaxf(m4[0], m4[1]), fmaxf(m4[2], m4[3]));
      float e[4];
#pragma unroll
      for (int r = 0; r < 4; ++r) e[r] = __builtin_amdgcn_exp2f((sc[r] - mx) * LOG2E);
      *(LAS v2u*)(L + L_P + hd * P_S + (32 * (2 * w + (dup >> 1)) + 8 * g + 4 * (dup & 1)) * 2) = v2u{pk(e[0], e[1]), pk(e[2], e[3])};
      float t = (e[0] + e[1]) + (e[2] + e[3]);
      t += dpp<ROR4>(t); t += dpp<ROR8>(t);
      t = x32_sum(x16_sum(t));
      if (lane < 4) ((LAS float*)(L + L_ST))[16 + lane * 4 + w] = t;
      flag_set(ldsb, F_P, w);
      flag_wait<4>(L, F_P);
      const f32x4 l4 = *(LAS f32x4*)(L + L_ST + 64 + hd * 16), l4g = *(LAS f32x4*)(L + L_ST + 64 + g * 16);
      inv = __builtin_amdgcn_rcpf((l4g[0] + l4g[1]) + (l4g[2] + l4g[3]));
      lse = fmaf(mx, LOG2E, __builtin_amdgcn_logf((l4[0] + l4[1]) + (l4[2] + l4[3])));
    }
    STAMP(6);
    {
      v4u pf[8];
#pragma unroll
      for (int u = 0; u < 8; ++u) pf[u] = ld128(L, L_P + dup * P_S + (32 * ((u + rot) & 7) + 8 * g) * 2);
      unsigned vb0 = L_V + rr * 256 + (((4 * w + (pp >> 1)) ^ ((rr & 7) << 1)) * 16) + 8 * (pp & 1);
      unsigned vb1 = L_V + rr * 256 + (((4 * w + 2 + (pp >> 1)) ^ ((rr & 7) << 1)) * 16) + 8 * (pp & 1);
      unsigned oaddr = coblk + g * 512 + w * 128 + i16 * 8;
      PIN(vb0); PIN(vb1); PIN(oaddr); PIN(inv); PIN(lse);
      if (w == 0 && lane < 4) gst64(ws, coblk + 2048 + lane * 8, v2u{__float_as_uint(lse), tag});
      f32x4 o0 = {0.f, 0.f, 0.f, 0.f}, o1 = {0.f, 0.f, 0.f, 0.f};
#pragma unroll
      for (int cc = 0; cc < 4; ++cc) {
        const unsigned ua = ((2 * cc + rot) & 7) * 8192, ub = ((2 * cc + 1 + rot) & 7) * 8192;
        unsigned a0 = vb0 + ua, a1 = vb1 + ua, b0 = vb0 + ub, b1 = vb1 + ub;
        PIN(a0); PIN(a1); PIN(b0); PIN(b1);
        flag_wait<2>(L, F_V + cc);
        const v4u a00 = tr_frag(L, a0, 4096), a10 = tr_frag(L, a1, 4096);
        const v4u a01 = tr_frag(L, b0, 4096), a11 = tr_frag(L, b1, 4096);
        __builtin_amdgcn_sched_barrier(0);
        o0 = mfma(pf[2 * cc], a00, o0); o1 = mfma(pf[2 * cc], a10, o1);
        o0 = mfma(pf[2 * cc + 1], a01, o0); o1 = mfma(pf[2 * cc + 1], a11, o1);
      }
      STAMP(7);
      if (roleB) flag_set(ldsb, F_PV, w);
      const float va = o0[0] * inv, vb = o1[0] * inv;
      const float na = dpp<XOR1>(va), nb = dpp<XOR1>(vb);
      gst64(ws, oaddr, v2u{(lane & 1) ? pk(nb, vb) : pk(va, na), tag});
    }
    STAMP(8);
  }

  if (roleA) {
    wg_barrier();
    tag = ((LAS unsigned*)(L + L_CT))[4 * F_TAG];
    STAMP(9);
    const int rr = 4 * g + (i16 >> 2), pp = i16 & 3;
    v4u af[4];
    {
      const unsigned ab = L_K + rr * 256 + (((2 * w + (pp >> 1)) ^ ((rr & 7) << 1)) * 16) + 8 * (pp & 1);
#pragma unroll
      for (int t = 0; t < 4; ++t) af[t] = tr_frag(L, ab + t * 8192, 4096);
    }
    const int br = j & 7, cb32 = (j >> 3) * 8 + hkv;
    const unsigned xr = ((const unsigned*)p.x)[br * 256 + cb32 * 16 + (w & 1) * 8 + (lane & 7)];
    unsigned rpaddr = WS_RP + (unsigned)((head * 8 + 4 * (g & 1) + (g >> 1)) * 512 + cb128 * 128 + 16 * w + i16) * 8;
    unsigned obase = L_O + (i16 & 7) * O_S + 8 * g * 2;
    PIN(rpaddr); PIN(obase);
    {
      const unsigned pb = WS_CO + (unsigned)(hkv * 32 + 4 * w) * CO_BLK;
      const unsigned o0 = pb + (lane >> 5) * CO_BLK + hl * 512 + (lane & 31) * 16, o1 = o0 + 2 * CO_BLK;
      const unsigned os = pb + (lane & 3) * CO_BLK + 2048 + hl * 8;
      const int ch = lane & 31;
      unsigned owr = L_O + w * O_S + (32 * (ch >> 3) + 8 * ((ch & 7) >> 1) + 2 * (ch & 1)) * 2;
      PIN(owr);
      v4u v0, v1; v2u st;
      if (CO_DLY) __builtin_amdgcn_s_sleep(CO_DLY);
      unsigned npass = 0;
      for (unsigned spins = 0;; ++spins) {
        asm volatile("" ::: "memory");
        v0 = gld128(ws, o0); v1 = gld128(ws, o1); st = gld64(ws, os);
        ++npass;
        if (__all(v0.y == tag && v0.w == tag && v1.y == tag && v1.w == tag && st.y == tag) || spins >= SPIN_CAP) break;
        if (CO_SLP) __builtin_amdgcn_s_sleep(CO_SLP);
      }
      COUNT(17, npass);
      STAMP(10);
      const float Ls = __uint_as_float(st.x);
      float Lm = fmaxf(Ls, dpp<XOR1>(Ls)); Lm = fmaxf(Lm, dpp<XOR2>(Lm));
      const float e = __builtin_amdgcn_exp2f(Ls - Lm);
      float es = e + dpp<XOR1>(e); es += dpp<XOR2>(es);
      const float wt = e * __builtin_amdgcn_rcpf(es);
      const float w0 = dpp<0x00>(wt), w1 = dpp<0x55>(wt), w2 = dpp<0xAA>(wt), w3 = dpp<0xFF>(wt);
      const float wa = lane < 32 ? w0 : w1, wb = lane < 32 ? w2 : w3;
      const float a0 = x32_sum(fmaf(wa, lo16(v0.x), wb * lo16(v1.x))), a1 = x32_sum(fmaf(wa, hi16(v0.x), wb * hi16(v1.x)));
      const float a2 = x32_sum(fmaf(wa, lo16(v0.z), wb * lo16(v1.z))), a3 = x32_sum(fmaf(wa, hi16(v0.z), wb * hi16(v1.z)));
      if (lane < 32) { *(LAS unsigned*)(L + owr) = pk(a0, a1); *(LAS unsigned*)(L + owr + 8) = pk(a2, a3); }
    }
    wg_barrier();
    {
      v4u bo[4];
#pragma unroll
      for (int t = 0; t < 4; ++t) bo[t] = ld128(L, obase + t * 64);
      f32x4 acc = {0.f, 0.f, 0.f, 0.f};
#pragma unroll
      for (int t = 0; t < 4; ++t) acc = mfma(bo[t], af[t], acc);
      gst64(ws, rpaddr, v2u{__float_as_uint(g < 2 ? acc[0] : acc[1]), tag});
      gst64(ws, rpaddr + 2 * 512 * 8, v2u{__float_as_uint(g < 2 ? acc[2] : acc[3]), tag});
    }
    STAMP(11);
    if (w < 2) {
      const int grp = lane >> 3, pr = lane & 7;
      float h0, h1;
      {
        const unsigned off = WS_RP + (unsigned)((grp * 8 + br) * 512 + 32 * cb32 + 16 * w + 2 * pr) * 8;
        v4u v[4];
        { const unsigned n = gather<4, R_DLY, R_SLP, R_MASK>(ws, off, 8u * 8 * 512 * 8, tag, v); COUNT(18, n); }
        STAMP(12);
        float s0 = (__uint_as_float(v[0].x) + __uint_as_float(v[1].x)) + (__uint_as_float(v[2].x) + __uint_as_float(v[3].x));
        float s1 = (__uint_as_float(v[0].z) + __uint_as_float(v[1].z)) + (__uint_as_float(v[2].z) + __uint_as_float(v[3].z));
        s0 += dpp<ROR8>(s0); s1 += dpp<ROR8>(s1);
        s0 = x32_sum(x16_sum(s0)); s1 = x32_sum(x16_sum(s1));
        h0 = rb(lo16(xr) + rb(s0)); h1 = rb(hi16(xr) + rb(s1));
        if (lane < 8) gst64(ws, WS_HG + (unsigned)(br * 256 + 16 * cb32 + 8 * w + lane) * 8, v2u{pk(h0, h1), tag});
      }
      STAMP(13);
    } else if (w == 2) {
      const int a = hkv * 16 + j, grp = lane >> 3, bb8 = lane & 7;
      v4u hv[1];
      gather<1, 8, 4, 0>(ws, WS_HG + (unsigned)(bb8 * 256 + 2 * a) * 8, 0, tag, hv);
      const unsigned off = WS_R2 + (unsigned)grp * 16384 + (a >> 3) * 1024 + (a & 3) * 256 + ((a >> 2) & 1) * 128 + bb8 * 16;
      v4u v[7];
      { const unsigned n = gather<7, R2_DLY, R2_SLP, R2_MASK>(ws, off, 8u * 16384, tag, v); COUNT(19, n); }
      STAMP(14);
      float s0 = lo16(v[0].x), s1 = hi16(v[0].x), s2 = lo16(v[0].z), s3 = hi16(v[0].z);
#pragma unroll
      for (int k = 1; k < 7; ++k) { s0 += lo16(v[k].x); s1 += hi16(v[k].x); s2 += lo16(v[k].z); s3 += hi16(v[k].z); }
      s0 += dpp<ROR8>(s0); s1 += dpp<ROR8>(s1); s2 += dpp<ROR8>(s2); s3 += dpp<ROR8>(s3);
      s0 = x32_sum(x16_sum(s0)); s1 = x32_sum(x16_sum(s1)); s2 = x32_sum(x16_sum(s2)); s3 = x32_sum(x16_sum(s3));
      if (lane < 8)
        ((v2u*)p.out)[bb8 * 128 + a] = v2u{pk(lo16(hv[0].x) + rb(s0), hi16(hv[0].x) + rb(s1)), pk(lo16(hv[0].z) + rb(s2), hi16(hv[0].z) + rb(s3))};
      if (c == 0 && lane == 0) __builtin_amdgcn_raw_buffer_store_b32(tag, ws, WS_E, 0, 16);
      STAMP(15);
    }
  } else if (roleB) {
    wg_barrier();
    tag = ((LAS unsigned*)(L + L_CT))[4 * F_TAG];
    STAMP(9);
    const int rr = 4 * g + (i16 >> 2), pp = i16 & 3;
    v4u df[4], gf[16];
    {
#pragma unroll
      for (int x = 0; x < 4; ++x)
        df[x] = tr_frag(L, L_V + rr * 1024 + (((2 * (4 * w + x) + (pp >> 1)) ^ ((rr & 7) << 1)) * 16) + 8 * (pp & 1), 16 * 1024);
      if (w < 4) {
        const int mat = pp >> 1;
        const unsigned gb = L_G + mat * 32768 + rr * 64 + ((w ^ (2 * ((rr >> 2) & 1) + mat)) * 16) + 8 * (pp & 1);
#pragma unroll
        for (int t = 0; t < 16; ++t) gf[t] = tr_frag(L, gb + t * 2048, 1024);
      }
    }
    unsigned r2addr = WS_R2 + (unsigned)m * 16384 + 2 * w * 1024 + lane * 16;
    unsigned bb = L_XN + (i16 & 7) * XN_S + 8 * g * 2;
    unsigned hnw = L_XN + w * XN_S + (32 * (lane >> 3) + 8 * (lane & 3) + 4 * ((lane >> 2) & 1)) * 2;
    unsigned hhr = L_HH + (i16 & 7) * HH_S + g * 16;
    unsigned hhw = L_HH + i16 * HH_S + (8 * ((2 * w + g) & 3) + 4 * ((2 * w + g) >> 2)) * 2;
    PIN(r2addr); PIN(bb); PIN(hnw); PIN(hhr); PIN(hhw);
    {
      const unsigned off = WS_HG + (unsigned)(w * 256 + 2 * lane) * 8;
      v4u hv[2];
      { const unsigned n = gather<2, H_DLY, H_SLP, H_MASK>(ws, off, 1024, tag, hv); COUNT(20, n); }
      const v4u v0 = hv[0], v1 = hv[1];
      STAMP(10);
      float v[8] = {lo16(v0.x), hi16(v0.x), lo16(v0.z), hi16(v0.z), lo16(v1.x), hi16(v1.x), lo16(v1.z), hi16(v1.z)};
      layernorm8(v);
      *(LAS v2u*)(L + hnw) = v2u{pk(v[0], v[1]), pk(v[2], v[3])};
      *(LAS v2u*)(L + hnw + 512) = v2u{pk(v[4], v[5]), pk(v[6], v[7])};
    }
    wg_barrier();
    STAMP(11);
    if (w < 4) {
      v4u hb[16];
#pragma unroll
      for (int t = 0; t < 16; ++t) hb[t] = ld128(L, bb + t * 64);
      __builtin_amdgcn_sched_barrier(0);
      f32x4 acc = {0.f, 0.f, 0.f, 0.f}, acc2 = {0.f, 0.f, 0.f, 0.f};
#pragma unroll
      for (int t = 0; t < 16; t += 2) { acc = mfma(gf[t], hb[t], acc); acc2 = mfma(gf[t + 1], hb[t + 1], acc2); }
      float hh[4];
#pragma unroll
      for (int r = 0; r < 4; ++r) {
        const float v = rb(acc[r] + acc2[r]);
        auto sw = __builtin_amdgcn_permlane32_swap(__float_as_uint(v), __float_as_uint(v), false, false);
        const float gt = __uint_as_float(sw[0]), up = __uint_as_float(sw[1]);
        const float sg = rb(__builtin_amdgcn_rcpf(1.0f + __builtin_amdgcn_exp2f(-gt * LOG2E)));
        hh[r] = sg * up;
      }
      if (lane < 32 && i16 < 8) *(LAS v2u*)(L + hhw) = v2u{pk(hh[0], hh[1]), pk(hh[2], hh[3])};
    }
    wg_barrier();
    STAMP(12);
    {
      const v4u bf = ld128(L, hhr);
      const int half = i16 >> 3;
      f32x4 acc[4];
#pragma unroll
      for (int x = 0; x < 4; ++x) acc[x] = mfma(df[x], bf, f32x4{0.f, 0.f, 0.f, 0.f});
      STAMP(14);
#pragma unroll
      for (int y = 0; y < 2; ++y) {
        const f32x4 e = acc[2 * y], d = acc[2 * y + 1];
        const float o0 = half ? d[0] : e[0], o1 = half ? d[1] : e[1], o2 = half ? d[2] : e[2], o3 = half ? d[3] : e[3];
        gst128(ws, r2addr + y * 1024, v4u{pk(o0, o1), tag, pk(o2, o3), tag});
      }
    }
    STAMP(13);
  }
}
}

extern "C" void kernel_launch(void* const* d_in, const int* in_sizes, int n_in, void* d_out, int out_size,
                              void* d_ws, size_t ws_size, hipStream_t stream) {
  fk::Params p;
  p.x = d_in[0]; p.kc = d_in[1]; p.vc = d_in[2]; p.wq = d_in[3]; p.wo = d_in[4];
  p.wg = d_in[6]; p.wu = d_in[7]; p.wd = d_in[8]; p.out = d_out; p.ws = d_ws;
  hipLaunchKernelGGL(fk::fused_decode, dim3(256), dim3(512), 0, stream, p);
}
```

```cpp
#define WARM_CODE 1
#include <hip/hip_runtime.h>
#include <hip/hip_bf16.h>
#include <stdint.h>

#define STAMPS 0
#define NT_KV 1
#define NT_W 1
#define PAD_T 0
#define WARM_TLB 0
#define WB_START 0
#define WB_MID 0
#define KV_WIN 12
#define Q_DELAY 8

#define Q_DLY 0
#define Q_SLP 2
#define Q_MASK 0
#define CO_DLY 0
#define CO_SLP 0
#define CO_MASK 0
#define R_DLY 0
#define R_SLP 0
#define R_MASK 0
#define H_DLY 0
#define H_SLP 0
#define H_MASK 0
#define R2_DLY 0
#define R2_SLP 1
#define R2_MASK 0

namespace fk {
#define LAS __attribute__((address_space(3)))
typedef unsigned v4u __attribute__((ext_vector_type(4)));
typedef unsigned v2u __attribute__((ext_vector_type(2)));
typedef float f32x4 __attribute__((ext_vector_type(4)));
typedef float f32x2 __attribute__((ext_vector_type(2)));
typedef short s16x4 __attribute__((ext_vector_type(4)));
typedef __bf16 bf16x8 __attribute__((ext_vector_type(8)));
typedef __bf16 bf16x2 __attribute__((ext_vector_type(2)));
typedef unsigned long long u64;
typedef __amdgpu_buffer_rsrc_t rsrc_t;

constexpr int NB = 8, DM = 512, NHKV = 8, SK = 4096, WIN = 1024, DH = 128, FF = 1792;
constexpr int NMLP = 56;
constexpr float SCALE = 0.08838834764831845f;
constexpr float LOG2E = 1.4426950408889634f;
constexpr unsigned SPIN_CAP = 1u << 15;

constexpr unsigned WS_E = 0;
constexpr unsigned WS_QG = 4096;
constexpr unsigned WS_CO = WS_QG + 8 * 8 * 4 * 512 * 8;
constexpr unsigned CO_BLK = 2176;
constexpr unsigned WS_RP = WS_CO + 256 * CO_BLK;
constexpr unsigned WS_HG = WS_RP + 32 * 8 * 512 * 8;
constexpr unsigned WS_R2 = WS_HG + 8 * 256 * 8;
constexpr unsigned WS_END = WS_R2 + NMLP * 16384;
constexpr unsigned WS_ST = 6u << 20;
constexpr unsigned WS_BYTES = STAMPS ? (8u << 20) : WS_END;

constexpr unsigned L_K = 0, L_V = 65536, L_WQ = 131072, L_XN = 147456, XN_S = 1056;
constexpr unsigned L_Q = L_XN + 8 * XN_S, Q_S = 320;
constexpr unsigned L_P = L_Q + 4 * Q_S, P_S = 576;
constexpr unsigned L_RED = L_P + 4 * P_S;
constexpr unsigned L_ST = L_RED + 2048;
constexpr unsigned L_CT = L_ST + 128;
constexpr unsigned LDS_BYTES = L_CT + 256;
static_assert(LDS_BYTES <= 163840, "LDS");
constexpr unsigned L_O = L_WQ, O_S = 288;
constexpr unsigned L_RR = L_WQ + 4096, L_RR2 = L_WQ + 6144;
constexpr unsigned L_HH = L_WQ + 8192, HH_S = 96;
constexpr unsigned L_G = L_K, L_U = L_K + 32768;
enum { F_XW = 0, F_Q = 1, F_MAX = 2, F_P = 3, F_PV = 4, F_K = 5, F_VI = 6, F_V = 7  , F_TAG = 11, F_KI = 12, F_N = 13 };

struct Params {
  const void* x; const void* kc; const void* vc; const void* wq; const void* wo;
  const void* wg; const void* wu; const void* wd; void* out; void* ws;
};

__device__ __forceinline__ rsrc_t mk_rsrc(const void* p, unsigned bytes) {
  return __builtin_amdgcn_make_buffer_rsrc((void*)p, 0, bytes, 0x00020000);
}
__device__ __forceinline__ unsigned pk(float a, float b) { bf16x2 v = {(__bf16)a, (__bf16)b}; return __builtin_bit_cast(unsigned, v); }
__device__ __forceinline__ float rb(float a) { return (float)(__bf16)a; }
__device__ __forceinline__ float lo16(unsigned u) { return __uint_as_float(u << 16); }
__device__ __forceinline__ float hi16(unsigned u) { return __uint_as_float(u & 0xffff0000u); }
__device__ __forceinline__ f32x4 mfma(v4u a, v4u b, f32x4 c) {
  return __builtin_amdgcn_mfma_f32_16x16x32_bf16(__builtin_bit_cast(bf16x8, a), __builtin_bit_cast(bf16x8, b), c, 0, 0, 0);
}
template <int CTRL> __device__ __forceinline__ float dpp(float x) {
  return __builtin_bit_cast(float, __builtin_amdgcn_mov_dpp(__builtin_bit_cast(int, x), CTRL, 0xf, 0xf, true));
}
constexpr int XOR1 = 0xB1, XOR2 = 0x4E, HMIR = 0x141, MIR = 0x140, ROR4 = 0x124, ROR8 = 0x128;
__device__ __forceinline__ float x16_sum(float x) {
  auto s = __builtin_amdgcn_permlane16_swap(__float_as_uint(x), __float_as_uint(x), false, false);
  return __uint_as_float(s[0]) + __uint_as_float(s[1]);
}
__device__ __forceinline__ float x32_sum(float x) {
  auto s = __builtin_amdgcn_permlane32_swap(__float_as_uint(x), __float_as_uint(x), false, false);
  return __uint_as_float(s[0]) + __uint_as_float(s[1]);
}
__device__ __forceinline__ float x16_max(float x) {
  auto s = __builtin_amdgcn_permlane16_swap(__float_as_uint(x), __float_as_uint(x), false, false);
  return fmaxf(__uint_as_float(s[0]), __uint_as_float(s[1]));
}
__device__ __forceinline__ float x32_max(float x) {
  auto s = __builtin_amdgcn_permlane32_swap(__float_as_uint(x), __float_as_uint(x), false, false);
  return fmaxf(__uint_as_float(s[0]), __uint_as_float(s[1]));
}
__device__ __forceinline__ float wave_sum(float x) {
  x += dpp<XOR1>(x); x += dpp<XOR2>(x); x += dpp<HMIR>(x); x += dpp<MIR>(x);
  return x32_sum(x16_sum(x));
}
__device__ __forceinline__ void layernorm8(float (&v)[8]) {
  const float pv = __uint_as_float(__builtin_amdgcn_readfirstlane(__float_as_uint(v[0])));
  float d[8];
#pragma unroll
  for (int i = 0; i < 8; ++i) d[i] = v[i] - pv;
  float s = ((d[0] + d[1]) + (d[2] + d[3])) + ((d[4] + d[5]) + (d[6] + d[7]));
  float q = fmaf(d[0], d[0], d[1] * d[1]) + fmaf(d[2], d[2], d[3] * d[3]) + (fmaf(d[4], d[4], d[5] * d[5]) + fmaf(d[6], d[6], d[7] * d[7]));
  s += dpp<XOR1>(s); q += dpp<XOR1>(q);
  s += dpp<XOR2>(s); q += dpp<XOR2>(q);
  s += dpp<HMIR>(s); q += dpp<HMIR>(q);
  s += dpp<MIR>(s);  q += dpp<MIR>(q);
  s = x16_sum(s); q = x16_sum(q);
  s = x32_sum(s) * (1.0f / 512.0f); q = x32_sum(q) * (1.0f / 512.0f);
  const float var = fmaxf(fmaf(-s, s, q), 0.f);
  const float mb = rb(pv + s);
  const float den = rb(__builtin_amdgcn_sqrtf(rb(rb(var) + 1e-5f)));
  const float inv = __builtin_amdgcn_rcpf(den);
#pragma unroll
  for (int i = 0; i < 8; ++i) v[i] = rb(v[i] - mb) * inv;
}

template <int IMM, int NT>
__device__ __forceinline__ void dma16(unsigned m0v, unsigned voff, rsrc_t srd, unsigned soff) {
  if constexpr (NT)
    asm volatile("s_mov_b32 m0, %0\n\ts_nop 0\n\tbuffer_load_dwordx4 %1, %2, %3 offen offset:%4 nt lds"
                 :: "s"(m0v), "v"(voff), "s"(srd), "s"(soff), "i"(IMM) : "memory");
  else
    asm volatile("s_mov_b32 m0, %0\n\ts_nop 0\n\tbuffer_load_dwordx4 %1, %2, %3 offen offset:%4 lds"
                 :: "s"(m0v), "v"(voff), "s"(srd), "s"(soff), "i"(IMM) : "memory");
}
#define VMWAIT(n) asm volatile("s_waitcnt vmcnt(" #n ")" ::: "memory")
template <int N> __device__ __forceinline__ void vmwait() { asm volatile("s_waitcnt vmcnt(%0)" :: "i"(N) : "memory"); }
#define LGKM0() asm volatile("s_waitcnt lgkmcnt(0)" ::: "memory")
__device__ __forceinline__ void wg_barrier() { LGKM0(); __builtin_amdgcn_s_barrier(); asm volatile("" ::: "memory"); }

__device__ __forceinline__ void flag_set(unsigned ldsb, int grp, int slot) {
  const unsigned a = ldsb + L_CT + grp * 16 + slot * 4, one = 1u;
  asm volatile("s_waitcnt lgkmcnt(0)\n\ts_mov_b64 exec, 1\n\tds_write_b32 %0, %1\n\ts_mov_b64 exec, -1" :: "v"(a), "v"(one) : "memory");
}
template <int N>
__device__ __forceinline__ void flag_wait(LAS unsigned char* L, int grp) {
  for (unsigned spins = 0; spins < (1u << 22); ++spins) {
    asm volatile("" ::: "memory");
    const v4u v = *(volatile LAS v4u*)(L + L_CT + grp * 16);
    const unsigned all = N == 4 ? (v.x & v.y & v.z & v.w) : (v.x & v.y);
    if (__builtin_amdgcn_readfirstlane(all)) break;
  }
  asm volatile("" ::: "memory");
}
#define PIN(x) asm volatile("" : "+v"(x))
__device__ __forceinline__ v4u ld128(LAS unsigned char* L, unsigned off) { return *(LAS v4u*)(L + off); }
__device__ __forceinline__ v4u tr_frag(LAS unsigned char* L, unsigned off, unsigned second) {
  const s16x4 a = __builtin_amdgcn_ds_read_tr16_b64_v4i16((LAS s16x4*)(L + off));
  const s16x4 b = __builtin_amdgcn_ds_read_tr16_b64_v4i16((LAS s16x4*)(L + off + second));
  const v2u x = __builtin_bit_cast(v2u, a), y = __builtin_bit_cast(v2u, b);
  return v4u{x.x, x.y, y.x, y.y};
}
__device__ __forceinline__ v4u gld128(rsrc_t rs, unsigned off) { return __builtin_amdgcn_raw_buffer_load_b128(rs, off, 0, 16); }
__device__ __forceinline__ v2u gld64(rsrc_t rs, unsigned off) { return __builtin_amdgcn_raw_buffer_load_b64(rs, off, 0, 16); }
__device__ __forceinline__ void gst128(rsrc_t rs, unsigned off, v4u v) { __builtin_amdgcn_raw_buffer_store_b128(v, rs, off, 0, 16); }
__device__ __forceinline__ void gst64(rsrc_t rs, unsigned off, v2u v) { __builtin_amdgcn_raw_buffer_store_b64(v, rs, off, 0, 16); }

template <int N, int DLY, int SLP, int MASK>
__device__ __forceinline__ unsigned gather(rsrc_t ws, unsigned off, unsigned stride, unsigned tag, v4u (&v)[N]) {
  if (DLY) __builtin_amdgcn_s_sleep(DLY);
  unsigned spins = 0;
  if (MASK) {
    bool need[N];
#pragma unroll
    for (int k = 0; k < N; ++k) need[k] = true;
    for (;; ++spins) {
      asm volatile("" ::: "memory");
#pragma unroll
      for (int k = 0; k < N; ++k) if (need[k]) v[k] = gld128(ws, off + k * stride);
      bool any = false;
#pragma unroll
      for (int k = 0; k < N; ++k) { if (need[k]) need[k] = !__all(v[k].y == tag && v[k].w == tag); any = any || need[k]; }
      if (!any || spins >= SPIN_CAP) break;
      if (SLP) __builtin_amdgcn_s_sleep(SLP);
    }
  } else {
    for (;; ++spins) {
      asm volatile("" ::: "memory");
#pragma unroll
      for (int k = 0; k < N; ++k) v[k] = gld128(ws, off + k * stride);
      bool ok = true;
#pragma unroll
      for (int k = 0; k < N; ++k) ok = ok && v[k].y == tag && v[k].w == tag;
      if (__all(ok) || spins >= SPIN_CAP) break;
      if (SLP) __builtin_amdgcn_s_sleep(SLP);
    }
  }
  return spins + 1;
}

#define STAMP(i) do {} while (0)
#define COUNT(i, v) do {} while (0)

__global__ void __launch_bounds__(512, 2) fused_decode(Params p) {
  __shared__ __attribute__((aligned(16))) unsigned char lds_raw[LDS_BYTES];
  LAS unsigned char* L = (LAS unsigned char*)lds_raw;
  const unsigned ldsb = (unsigned)(size_t)L;
  STAMP(0);
  u64 code_base; asm volatile("s_getpc_b64 %0" : "=s"(code_base));
  code_base &= ~(u64)255;
  asm volatile("" :: "s"(p.x), "s"(p.kc), "s"(p.vc), "s"(p.wq), "s"(p.wo), "s"(p.wg), "s"(p.wu), "s"(p.wd), "s"(p.out), "s"(p.ws));
  const int tid = threadIdx.x, lane = tid & 63;
  const int w = __builtin_amdgcn_readfirstlane(tid >> 6);
  const int c = blockIdx.x, hkv = c & 7, j = c >> 3, b = j >> 2, s = j & 3;
  const int g = lane >> 4, i16 = lane & 15;
  const rsrc_t ws = mk_rsrc(p.ws, WS_BYTES);
  unsigned tag = 0;
  const bool roleA = j < 16;
  const int hi4 = hkv >> 2;
  const bool roleB = j >= 16 && j - 16 < 8 - 2 * hi4;
  const int m = (8 - 2 * hi4) * hkv + 8 * hi4 + (j - 16);
  const int hl = j >> 2, cb128 = j & 3, head = 4 * hkv + hl;
  const unsigned coblk = WS_CO + (unsigned)(hkv * 32 + j) * CO_BLK;
  const int rot = (j + 3 * hkv) & 7;

  if (w >= 6) {
    if (!(roleA || roleB)) {
      if (j == 29) {
        typedef __attribute__((address_space(1))) const unsigned gcu;
        unsigned acc = 0;
#pragma unroll
        for (int q = 0; q < 2; ++q) {
          const unsigned o = (unsigned)((w - 6) * 2 + q) * 8192u + lane * 128u;
          if (o < 20480u) acc += *(gcu*)(code_base + o);
        }
        asm volatile("s_waitcnt vmcnt(0)" :: "v"(acc) : "memory");
      }
      return;
    }
    const int l = w - 6;
    __builtin_amdgcn_s_barrier();
    asm volatile("" ::: "memory");
    if (roleA) {
      const rsrc_t ro = mk_rsrc((const char*)p.wo + (size_t)head * 128 * 1024 + cb128 * 256, 128 * 1024 - cb128 * 256);
      unsigned vo[2];
#pragma unroll
      for (int r = 0; r < 2; ++r) vo[r] = g * 1024 + ((i16 ^ (((4 * r + g) & 7) << 1)) * 16);
      flag_wait<2>(L, F_VI);
      flag_wait<4>(L, F_MAX);
      STAMP(1);
#pragma unroll
      for (int q = 0; q < 16; ++q) dma16<0, NT_W>(ldsb + L_K + (64 * l + 4 * q) * 256, vo[q & 1], ro, (64 * l + 4 * q) * 1024);
      STAMP(2);
      VMWAIT(0);
    } else {
      const rsrc_t rg = mk_rsrc((const char*)(l == 0 ? p.wg : p.wu) + m * 64, 512 * FF * 2 - m * 64);
      const unsigned vg = (lane >> 2) * (FF * 2) + (((lane & 3) ^ (2 * ((lane >> 4) & 1) + l)) * 16);
      const rsrc_t rd = mk_rsrc((const char*)p.wd + (size_t)m * 32 * 1024, 32 * 1024);
      flag_wait<2>(L, F_VI);
      flag_wait<4>(L, F_MAX);
      STAMP(1);
#pragma unroll
      for (int q = 0; q < 32; ++q) {
        const unsigned qq = (q + 5 * m) & 31;
        dma16<0, NT_W>(ldsb + L_G + l * 32768 + qq * 1024, vg, rg, qq * 16 * (FF * 2));
      }
      STAMP(2);
      flag_wait<4>(L, F_PV);
#pragma unroll
      for (int q = 0; q < 16; ++q) dma16<0, NT_W>(ldsb + L_V + (16 * l + q) * 1024, (unsigned)(lane ^ ((q & 7) << 1)) * 16, rd, (16 * l + q) * 1024);
      STAMP(3);
      VMWAIT(0);
    }
    STAMP(4);
  } else if (w >= 4) {
    const int l = w - 4;
    const size_t win = ((size_t)(b * NHKV + hkv) * SK + (SK - WIN) + 256 * s) * (DH * 2);
    const rsrc_t rk = mk_rsrc((const char*)p.kc + win, 65536), rv = mk_rsrc((const char*)p.vc + win, 65536);
    unsigned vk[4], vv[2];
#pragma unroll
    for (int r = 0; r < 4; ++r) vk[r] = g * 256 + ((i16 ^ (4 * r + g)) * 16);
#pragma unroll
    for (int r = 0; r < 2; ++r) vv[r] = g * 256 + ((i16 ^ (((4 * r + g) & 7) << 1)) * 16);
    asm volatile("s_nop 4" ::: "memory");
#define WIN_WAIT() vmwait<KV_WIN - 1>()
#define GOFF(a) ((unsigned)(32 * (((a) + rot) & 7) + 16 * l) * 256)
#define K_ONE(a, r) dma16<1024 * (r), NT_KV>(ldsb + L_K + GOFF(a), vk[r], rk, GOFF(a))
#define V_ONE(a, r) dma16<1024 * (r), NT_KV>(ldsb + L_V + GOFF(a), vv[(r) & 1], rv, GOFF(a))
#define K_GROUP(a) { WIN_WAIT(); K_ONE(a, 0); WIN_WAIT(); K_ONE(a, 1); WIN_WAIT(); K_ONE(a, 2); WIN_WAIT(); K_ONE(a, 3); }
#define V_GROUP(a) { WIN_WAIT(); V_ONE(a, 0); WIN_WAIT(); V_ONE(a, 1); WIN_WAIT(); V_ONE(a, 2); WIN_WAIT(); V_ONE(a, 3); }
    K_ONE(0, 0); K_ONE(0, 1); K_ONE(0, 2); K_ONE(0, 3);
    __builtin_amdgcn_s_barrier();
    asm volatile("" ::: "memory");
    VMWAIT(3);
    K_GROUP(1) K_GROUP(2) K_GROUP(3) K_GROUP(4) K_GROUP(5) K_GROUP(6) K_GROUP(7)
    flag_set(ldsb, F_KI, l);
    STAMP(1);
    V_GROUP(0) V_GROUP(1) V_GROUP(2)
    vmwait<(KV_WIN < 12 ? KV_WIN : 12)>();
    flag_set(ldsb, F_K, l);
    STAMP(2);
    V_GROUP(3) V_GROUP(4) V_GROUP(5) V_GROUP(6) V_GROUP(7)
    flag_set(ldsb, F_VI, l);
    STAMP(3);
    if (KV_WIN > 24) { VMWAIT(24); }
    flag_set(ldsb, F_V + 0, l);
    if (KV_WIN > 16) { VMWAIT(16); }
    flag_set(ldsb, F_V + 1, l);
    VMWAIT(8);  flag_set(ldsb, F_V + 2, l);
    VMWAIT(0);  flag_set(ldsb, F_V + 3, l);
    STAMP(4);
  } else {
    const int qcb = j & 7, kq = j >> 3;
    {
      const rsrc_t rx = mk_rsrc(p.x, NB * DM * 2);
      const int n0 = hkv * 512 + 64 * qcb;
      const rsrc_t rq = mk_rsrc((const char*)p.wq + (size_t)kq * 128 * 8192 + n0 * 2, 128 * 8192 - n0 * 2);
      const unsigned vq = (lane >> 3) * 8192 + (((lane & 7) ^ (((lane >> 4) & 3) << 1)) * 16);
      asm volatile("s_nop 4\n\tbuffer_load_dword %0, off, %1, 0 sc1" : "=&v"(tag) : "s"(ws) : "memory");
      dma16<0, 0>(ldsb + L_XN + (2 * w) * XN_S, lane * 16, rx, (2 * w) * 1024);
      dma16<0, 0>(ldsb + L_XN + (2 * w + 1) * XN_S, lane * 16, rx, (2 * w + 1) * 1024);
#pragma unroll
      for (int q = 0; q < 4; ++q) dma16<0, NT_W>(ldsb + L_WQ + (32 * w + 8 * q) * 128, vq, rq, (32 * w + 8 * q) * 8192);
    }
    if (w == 0 && lane < 4 * F_N) ((LAS unsigned*)(L + L_CT))[lane] = 0u;
    wg_barrier();
    asm volatile("s_waitcnt vmcnt(4)" : "+v"(tag) :: "memory");
    STAMP(1);
    tag += 1u;
    if (w == 0 && lane == 0) ((LAS unsigned*)(L + L_CT))[4 * F_TAG] = tag;
    {
      const v4u raw0 = ld128(L, L_XN + (2 * w) * XN_S + lane * 16), raw1 = ld128(L, L_XN + (2 * w + 1) * XN_S + lane * 16);
      float v0[8] = {lo16(raw0.x), hi16(raw0.x), lo16(raw0.y), hi16(raw0.y), lo16(raw0.z), hi16(raw0.z), lo16(raw0.w), hi16(raw0.w)};
      float v1[8] = {lo16(raw1.x), hi16(raw1.x), lo16(raw1.y), hi16(raw1.y), lo16(raw1.z), hi16(raw1.z), lo16(raw1.w), hi16(raw1.w)};
      layernorm8(v0); layernorm8(v1);
      const int t = lane >> 2, q = lane & 3;
      const unsigned base = L_XN + (2 * w) * XN_S + (32 * t + 16 * (q & 1) + 4 * (q >> 1)) * 2;
      asm volatile("" ::: "memory");
      *(LAS v2u*)(L + base) = v2u{pk(v0[0], v0[1]), pk(v0[2], v0[3])};
      *(LAS v2u*)(L + base + 16) = v2u{pk(v0[4], v0[5]), pk(v0[6], v0[7])};
      *(LAS v2u*)(L + base + XN_S) = v2u{pk(v1[0], v1[1]), pk(v1[2], v1[3])};
      *(LAS v2u*)(L + base + XN_S + 16) = v2u{pk(v1[4], v1[5]), pk(v1[6], v1[7])};
    }
    VMWAIT(0);
    STAMP(2);
    flag_set(ldsb, F_XW, w);
    flag_wait<4>(L, F_XW);
    const int rr = 4 * g + (i16 >> 2), pp = i16 & 3;
    {
      const unsigned ab = L_WQ + rr * 128 + (((2 * w + (pp >> 1)) ^ (((rr >> 1) & 3) << 1)) * 16) + 8 * (pp & 1);
      const unsigned bb = L_XN + (i16 & 7) * XN_S + (128 * kq + 8 * g) * 2;
      v4u a[4], bq[4];
#pragma unroll
      for (int t = 0; t < 4; ++t) { a[t] = tr_frag(L, ab + t * 4096, 2048); bq[t] = ld128(L, bb + t * 64); }
      f32x4 acc = {0.f, 0.f, 0.f, 0.f};
#pragma unroll
      for (int t = 0; t < 4; ++t) acc = mfma(bq[t], a[t], acc);
      const int b1 = 4 * (g & 1) + (g >> 1);
      const unsigned qa = WS_QG + (unsigned)(((hkv * 8 + b1) * 4 + kq) * 512 + 64 * qcb + 16 * w + i16) * 8;
      gst64(ws, qa, v2u{__float_as_uint(g < 2 ? acc[0] : acc[1]), tag});
      gst64(ws, qa + 2 * 4 * 512 * 8, v2u{__float_as_uint(g < 2 ? acc[2] : acc[3]), tag});
    }
    STAMP(3);
    {
      const unsigned off = WS_QG + (unsigned)((hkv * 8 + b) * 4 * 512 + 128 * w + 2 * lane) * 8;
      v4u v[4];
      flag_wait<2>(L, F_KI);
      { const unsigned n = gather<4, Q_DLY, Q_SLP, Q_MASK>(ws, off, 4096, tag, v); COUNT(16, n); }
      const float q0 = (__uint_as_float(v[0].x) + __uint_as_float(v[1].x)) + (__uint_as_float(v[2].x) + __uint_as_float(v[3].x));
      const float q1 = (__uint_as_float(v[0].z) + __uint_as_float(v[1].z)) + (__uint_as_float(v[2].z) + __uint_as_float(v[3].z));
      *(LAS unsigned*)(L + L_Q + w * Q_S + lane * 4) = pk(q0, q1);
    }
    flag_set(ldsb, F_Q, w);
    flag_wait<4>(L, F_Q);
    STAMP(4);
    float sc[4];
    const int hd = i16 & 3, dup = i16 >> 2;
    {
      v4u qf[4];
#pragma unroll
      for (int t = 0; t < 4; ++t) qf[t] = ld128(L, L_Q + hd * Q_S + (4 * t + g) * 16);
      flag_wait<2>(L, F_K);
      STAMP(5);
      v4u kf[16];
#pragma unroll
      for (int T = 0; T < 4; ++T)
#pragma unroll
        for (int t = 0; t < 4; ++t) kf[4 * T + t] = ld128(L, L_K + (64 * w + 16 * T + i16) * 256 + (((4 * t + g) ^ i16) * 16));
      __builtin_amdgcn_sched_barrier(0);
      f32x4 acc[4];
#pragma unroll
      for (int T = 0; T < 4; ++T) acc[T] = f32x4{0.f, 0.f, 0.f, 0.f};
#pragma unroll
      for (int t = 0; t < 4; ++t)
#pragma unroll
        for (int T = 0; T < 4; ++T) acc[T] = mfma(kf[4 * T + t], qf[t], acc[T]);
#pragma unroll
      for (int r = 0; r < 4; ++r) {
        const float a01 = (dup & 1) ? acc[1][r] : acc[0][r], a23 = (dup & 1) ? acc[3][r] : acc[2][r];
        sc[r] = rb(rb((dup & 2) ? a23 : a01) * SCALE);
      }
    }
    float mx, inv, lse;
    {
      float v = fmaxf(fmaxf(sc[0], sc[1]), fmaxf(sc[2], sc[3]));
      v = fmaxf(v, dpp<ROR4>(v)); v = fmaxf(v, dpp<ROR8>(v));
      v = x32_max(x16_max(v));
      if (lane < 4) ((LAS float*)(L + L_ST))[lane * 4 + w] = v;
      flag_set(ldsb, F_MAX, w);
      flag_wait<4>(L, F_MAX);
      const f32x4 m4 = *(LAS f32x4*)(L + L_ST + hd * 16);
      mx = fmaxf(fmaxf(m4[0], m4[1]), fmaxf(m4[2], m4[3]));
      float e[4];
#pragma unroll
      for (int r = 0; r < 4; ++r) e[r] = __builtin_amdgcn_exp2f((sc[r] - mx) * LOG2E);
      *(LAS v2u*)(L + L_P + hd * P_S + (32 * (2 * w + (dup >> 1)) + 8 * g + 4 * (dup & 1)) * 2) = v2u{pk(e[0], e[1]), pk(e[2], e[3])};
      float t = (e[0] + e[1]) + (e[2] + e[3]);
      t += dpp<ROR4>(t); t += dpp<ROR8>(t);
      t = x32_sum(x16_sum(t));
      if (lane < 4) ((LAS float*)(L + L_ST))[16 + lane * 4 + w] = t;
      flag_set(ldsb, F_P, w);
      flag_wait<4>(L, F_P);
      const f32x4 l4 = *(LAS f32x4*)(L + L_ST + 64 + hd * 16), l4g = *(LAS f32x4*)(L + L_ST + 64 + g * 16);
      inv = __builtin_amdgcn_rcpf((l4g[0] + l4g[1]) + (l4g[2] + l4g[3]));
      lse = fmaf(mx, LOG2E, __builtin_amdgcn_logf((l4[0] + l4[1]) + (l4[2] + l4[3])));
    }
    STAMP(6);
    {
      v4u pf[8];
#pragma unroll
      for (int u = 0; u < 8; ++u) pf[u] = ld128(L, L_P + dup * P_S + (32 * ((u + rot) & 7) + 8 * g) * 2);
      unsigned vb0 = L_V + rr * 256 + (((4 * w + (pp >> 1)) ^ ((rr & 7) << 1)) * 16) + 8 * (pp & 1);
      unsigned vb1 = L_V + rr * 256 + (((4 * w + 2 + (pp >> 1)) ^ ((rr & 7) << 1)) * 16) + 8 * (pp & 1);
      unsigned oaddr = coblk + g * 512 + w * 128 + i16 * 8;
      PIN(vb0); PIN(vb1); PIN(oaddr); PIN(inv); PIN(lse);
      if (w == 0 && lane < 4) gst64(ws, coblk + 2048 + lane * 8, v2u{__float_as_uint(lse), tag});
      f32x4 o0 = {0.f, 0.f, 0.f, 0.f}, o1 = {0.f, 0.f, 0.f, 0.f};
#pragma unroll
      for (int cc = 0; cc < 4; ++cc) {
        const unsigned ua = ((2 * cc + rot) & 7) * 8192, ub = ((2 * cc + 1 + rot) & 7) * 8192;
        unsigned a0 = vb0 + ua, a1 = vb1 + ua, b0 = vb0 + ub, b1 = vb1 + ub;
        PIN(a0); PIN(a1); PIN(b0); PIN(b1);
        flag_wait<2>(L, F_V + cc);
        const v4u a00 = tr_frag(L, a0, 4096), a10 = tr_frag(L, a1, 4096);
        const v4u a01 = tr_frag(L, b0, 4096), a11 = tr_frag(L, b1, 4096);
        __builtin_amdgcn_sched_barrier(0);
        o0 = mfma(pf[2 * cc], a00, o0); o1 = mfma(pf[2 * cc], a10, o1);
        o0 = mfma(pf[2 * cc + 1], a01, o0); o1 = mfma(pf[2 * cc + 1], a11, o1);
      }
      STAMP(7);
      if (roleB) flag_set(ldsb, F_PV, w);
      const float va = o0[0] * inv, vb = o1[0] * inv;
      const float na = dpp<XOR1>(va), nb = dpp<XOR1>(vb);
      gst64(ws, oaddr, v2u{(lane & 1) ? pk(nb, vb) : pk(va, na), tag});
    }
    STAMP(8);
  }

  if (roleA) {
    wg_barrier();
    tag = ((LAS unsigned*)(L + L_CT))[4 * F_TAG];
    STAMP(9);
    const int rr = 4 * g + (i16 >> 2), pp = i16 & 3;
    v4u af[4];
    {
      const unsigned ab = L_K + rr * 256 + (((2 * w + (pp >> 1)) ^ ((rr & 7) << 1)) * 16) + 8 * (pp & 1);
#pragma unroll
      for (int t = 0; t < 4; ++t) af[t] = tr_frag(L, ab + t * 8192, 4096);
    }
    const int br = j & 7, cb32 = (j >> 3) * 8 + hkv;
    const unsigned xr = ((const unsigned*)p.x)[br * 256 + cb32 * 16 + (w & 1) * 8 + (lane & 7)];
    unsigned rpaddr = WS_RP + (unsigned)((head * 8 + 4 * (g & 1) + (g >> 1)) * 512 + cb128 * 128 + 16 * w + i16) * 8;
    unsigned obase = L_O + (i16 & 7) * O_S + 8 * g * 2;
    PIN(rpaddr); PIN(obase);
    {
      const unsigned pb = WS_CO + (unsigned)(hkv * 32 + 4 * w) * CO_BLK;
      const unsigned o0 = pb + (lane >> 5) * CO_BLK + hl * 512 + (lane & 31) * 16, o1 = o0 + 2 * CO_BLK;
      const unsigned os = pb + (lane & 3) * CO_BLK + 2048 + hl * 8;
      const int ch = lane & 31;
      unsigned owr = L_O + w * O_S + (32 * (ch >> 3) + 8 * ((ch & 7) >> 1) + 2 * (ch & 1)) * 2;
      PIN(owr);
      v4u v0, v1; v2u st;
      if (CO_DLY) __builtin_amdgcn_s_sleep(CO_DLY);
      unsigned npass = 0;
      for (unsigned spins = 0;; ++spins) {
        asm volatile("" ::: "memory");
        v0 = gld128(ws, o0); v1 = gld128(ws, o1); st = gld64(ws, os);
        ++npass;
        if (__all(v0.y == tag && v0.w == tag && v1.y == tag && v1.w == tag && st.y == tag) || spins >= SPIN_CAP) break;
        if (CO_SLP) __builtin_amdgcn_s_sleep(CO_SLP);
      }
      COUNT(17, npass);
      STAMP(10);
      const float Ls = __uint_as_float(st.x);
      float Lm = fmaxf(Ls, dpp<XOR1>(Ls)); Lm = fmaxf(Lm, dpp<XOR2>(Lm));
      const float e = __builtin_amdgcn_exp2f(Ls - Lm);
      float es = e + dpp<XOR1>(e); es += dpp<XOR2>(es);
      const float wt = e * __builtin_amdgcn_rcpf(es);
      const float w0 = dpp<0x00>(wt), w1 = dpp<0x55>(wt), w2 = dpp<0xAA>(wt), w3 = dpp<0xFF>(wt);
      const float wa = lane < 32 ? w0 : w1, wb = lane < 32 ? w2 : w3;
      const float a0 = x32_sum(fmaf(wa, lo16(v0.x), wb * lo16(v1.x))), a1 = x32_sum(fmaf(wa, hi16(v0.x), wb * hi16(v1.x)));
      const float a2 = x32_sum(fmaf(wa, lo16(v0.z), wb * lo16(v1.z))), a3 = x32_sum(fmaf(wa, hi16(v0.z), wb * hi16(v1.z)));
      if (lane < 32) { *(LAS unsigned*)(L + owr) = pk(a0, a1); *(LAS unsigned*)(L + owr + 8) = pk(a2, a3); }
    }
    wg_barrier();
    {
      v4u bo[4];
#pragma unroll
      for (int t = 0; t < 4; ++t) bo[t] = ld128(L, obase + t * 64);
      f32x4 acc = {0.f, 0.f, 0.f, 0.f};
#pragma unroll
      for (int t = 0; t < 4; ++t) acc = mfma(bo[t], af[t], acc);
      gst64(ws, rpaddr, v2u{__float_as_uint(g < 2 ? acc[0] : acc[1]), tag});
      gst64(ws, rpaddr + 2 * 512 * 8, v2u{__float_as_uint(g < 2 ? acc[2] : acc[3]), tag});
    }
    STAMP(11);
    if (w < 2) {
      const int grp = lane >> 3, pr = lane & 7;
      float h0, h1;
      {
        const unsigned off = WS_RP + (unsigned)((grp * 8 + br) * 512 + 32 * cb32 + 16 * w + 2 * pr) * 8;
        v4u v[4];
        { const unsigned n = gather<4, R_DLY, R_SLP, R_MASK>(ws, off, 8u * 8 * 512 * 8, tag, v); COUNT(18, n); }
        STAMP(12);
        float s0 = (__uint_as_float(v[0].x) + __uint_as_float(v[1].x)) + (__uint_as_float(v[2].x) + __uint_as_float(v[3].x));
        float s1 = (__uint_as_float(v[0].z) + __uint_as_float(v[1].z)) + (__uint_as_float(v[2].z) + __uint_as_float(v[3].z));
        s0 += dpp<ROR8>(s0); s1 += dpp<ROR8>(s1);
        s0 = x32_sum(x16_sum(s0)); s1 = x32_sum(x16_sum(s1));
        h0 = rb(lo16(xr) + rb(s0)); h1 = rb(hi16(xr) + rb(s1));
        if (lane < 8) gst64(ws, WS_HG + (unsigned)(br * 256 + 16 * cb32 + 8 * w + lane) * 8, v2u{pk(h0, h1), tag});
      }
      STAMP(13);
    } else if (w == 2) {
      const int a = hkv * 16 + j, grp = lane >> 3, bb8 = lane & 7;
      v4u hv[1];
      gather<1, 8, 4, 0>(ws, WS_HG + (unsigned)(bb8 * 256 + 2 * a) * 8, 0, tag, hv);
      const unsigned off = WS_R2 + (unsigned)grp * 16384 + (a >> 3) * 1024 + (a & 3) * 256 + ((a >> 2) & 1) * 128 + bb8 * 16;
      v4u v[7];
      { const unsigned n = gather<7, R2_DLY, R2_SLP, R2_MASK>(ws, off, 8u * 16384, tag, v); COUNT(19, n); }
      STAMP(14);
      float s0 = lo16(v[0].x), s1 = hi16(v[0].x), s2 = lo16(v[0].z), s3 = hi16(v[0].z);
#pragma unroll
      for (int k = 1; k < 7; ++k) { s0 += lo16(v[k].x); s1 += hi16(v[k].x); s2 += lo16(v[k].z); s3 += hi16(v[k].z); }
      s0 += dpp<ROR8>(s0); s1 += dpp<ROR8>(s1); s2 += dpp<ROR8>(s2); s3 += dpp<ROR8>(s3);
      s0 = x32_sum(x16_sum(s0)); s1 = x32_sum(x16_sum(s1)); s2 = x32_sum(x16_sum(s2)); s3 = x32_sum(x16_sum(s3));
      if (lane < 8)
        ((v2u*)p.out)[bb8 * 128 + a] = v2u{pk(lo16(hv[0].x) + rb(s0), hi16(hv[0].x) + rb(s1)), pk(lo16(hv[0].z) + rb(s2), hi16(hv[0].z) + rb(s3))};
      if (c == 0 && lane == 0) __builtin_amdgcn_raw_buffer_store_b32(tag, ws, WS_E, 0, 16);
      STAMP(15);
    }
  } else if (roleB) {
    wg_barrier();
    tag = ((LAS unsigned*)(L + L_CT))[4 * F_TAG];
    STAMP(9);
    const int rr = 4 * g + (i16 >> 2), pp = i16 & 3;
    v4u df[4], gf[16];
    {
#pragma unroll
      for (int x = 0; x < 4; ++x)
        df[x] = tr_frag(L, L_V + rr * 1024 + (((2 * (4 * w + x) + (pp >> 1)) ^ ((rr & 7) << 1)) * 16) + 8 * (pp & 1), 16 * 1024);
      if (w < 4) {
        const int mat = pp >> 1;
        const unsigned gb = L_G + mat * 32768 + rr * 64 + ((w ^ (2 * ((rr >> 2) & 1) + mat)) * 16) + 8 * (pp & 1);
#pragma unroll
        for (int t = 0; t < 16; ++t) gf[t] = tr_frag(L, gb + t * 2048, 1024);
      }
    }
    unsigned r2addr = WS_R2 + (unsigned)m * 16384 + 2 * w * 1024 + lane * 16;
    unsigned bb = L_XN + (i16 & 7) * XN_S + 8 * g * 2;
    unsigned hnw = L_XN + w * XN_S + (32 * (lane >> 3) + 8 * (lane & 3) + 4 * ((lane >> 2) & 1)) * 2;
    unsigned hhr = L_HH + (i16 & 7) * HH_S + g * 16;
    unsigned hhw = L_HH + i16 * HH_S + (8 * ((2 * w + g) & 3) + 4 * ((2 * w + g) >> 2)) * 2;
    PIN(r2addr); PIN(bb); PIN(hnw); PIN(hhr); PIN(hhw);
    {
      const unsigned off = WS_HG + (unsigned)(w * 256 + 2 * lane) * 8;
      v4u hv[2];
      { const unsigned n = gather<2, H_DLY, H_SLP, H_MASK>(ws, off, 1024, tag, hv); COUNT(20, n); }
      const v4u v0 = hv[0], v1 = hv[1];
      STAMP(10);
      float v[8] = {lo16(v0.x), hi16(v0.x), lo16(v0.z), hi16(v0.z), lo16(v1.x), hi16(v1.x), lo16(v1.z), hi16(v1.z)};
      layernorm8(v);
      *(LAS v2u*)(L + hnw) = v2u{pk(v[0], v[1]), pk(v[2], v[3])};
      *(LAS v2u*)(L + hnw + 512) = v2u{pk(v[4], v[5]), pk(v[6], v[7])};
    }
    wg_barrier();
    STAMP(11);
    if (w < 4) {
      v4u hb[16];
#pragma unroll
      for (int t = 0; t < 16; ++t) hb[t] = ld128(L, bb + t * 64);
      __builtin_amdgcn_sched_barrier(0);
      f32x4 acc = {0.f, 0.f, 0.f, 0.f}, acc2 = {0.f, 0.f, 0.f, 0.f};
#pragma unroll
      for (int t = 0; t < 16; t += 2) { acc = mfma(gf[t], hb[t], acc); acc2 = mfma(gf[t + 1], hb[t + 1], acc2); }
      float hh[4];
#pragma unroll
      for (int r = 0; r < 4; ++r) {
        const float v = rb(acc[r] + acc2[r]);
        auto sw = __builtin_amdgcn_permlane32_swap(__float_as_uint(v), __float_as_uint(v), false, false);
        const float gt = __uint_as_float(sw[0]), up = __uint_as_float(sw[1]);
        const float sg = rb(__builtin_amdgcn_rcpf(1.0f + __builtin_amdgcn_exp2f(-gt * LOG2E)));
        hh[r] = sg * up;
      }
      if (lane < 32 && i16 < 8) *(LAS v2u*)(L + hhw) = v2u{pk(hh[0], hh[1]), pk(hh[2], hh[3])};
    }
    wg_barrier();
    STAMP(12);
    {
      const v4u bf = ld128(L, hhr);
      const int half = i16 >> 3;
      f32x4 acc[4];
#pragma unroll
      for (int x = 0; x < 4; ++x) acc[x] = mfma(df[x], bf, f32x4{0.f, 0.f, 0.f, 0.f});
      STAMP(14);
#pragma unroll
      for (int y = 0; y < 2; ++y) {
        const f32x4 e = acc[2 * y], d = acc[2 * y + 1];
        const float o0 = half ? d[0] : e[0], o1 = half ? d[1] : e[1], o2 = half ? d[2] : e[2], o3 = half ? d[3] : e[3];
        gst128(ws, r2addr + y * 1024, v4u{pk(o0, o1), tag, pk(o2, o3), tag});
      }
    }
    STAMP(13);
  }
}
}

extern "C" void kernel_launch(void* const* d_in, const int* in_sizes, int n_in, void* d_out, int out_size,
                              void* d_ws, size_t ws_size, hipStream_t stream) {
  fk::Params p;
  p.x = d_in[0]; p.kc = d_in[1]; p.vc = d_in[2]; p.wq = d_in[3]; p.wo = d_in[4];
  p.wg = d_in[6]; p.wu = d_in[7]; p.wd = d_in[8]; p.out = d_out; p.ws = d_ws;
  hipLaunchKernelGGL(fk::fused_decode, dim3(256), dim3(512), 0, stream, p);
}
```

```cpp
#define WARM_CODE 1
#define EXPT 3
#include <hip/hip_runtime.h>
#include <hip/hip_bf16.h>
#include <stdint.h>

#define STAMPS 0
#define NT_KV 1
#define NT_W 1
#define PAD_T 0
#define WARM_TLB 0
#define WB_START 0
#define WB_MID 0
#define KV_WIN 12
#define Q_DELAY 8

#define Q_DLY 0
#define Q_SLP 2
#define Q_MASK 0
#define CO_DLY 0
#define CO_SLP 0
#define CO_MASK 0
#define R_DLY 0
#define R_SLP 0
#define R_MASK 0
#define H_DLY 0
#define H_SLP 0
#define H_MASK 0
#define R2_DLY 0
#define R2_SLP 1
#define R2_MASK 0

namespace fk {
#define LAS __attribute__((address_space(3)))
typedef unsigned v4u __attribute__((ext_vector_type(4)));
typedef unsigned v2u __attribute__((ext_vector_type(2)));
typedef float f32x4 __attribute__((ext_vector_type(4)));
typedef float f32x2 __attribute__((ext_vector_type(2)));
typedef short s16x4 __attribute__((ext_vector_type(4)));
typedef __bf16 bf16x8 __attribute__((ext_vector_type(8)));
typedef __bf16 bf16x2 __attribute__((ext_vector_type(2)));
typedef unsigned long long u64;
typedef __amdgpu_buffer_rsrc_t rsrc_t;

constexpr int NB = 8, DM = 512, NHKV = 8, SK = 4096, WIN = 1024, DH = 128, FF = 1792;
constexpr int NMLP = 56;
constexpr float SCALE = 0.08838834764831845f;
constexpr float LOG2E = 1.4426950408889634f;
constexpr unsigned SPIN_CAP = 1u << 15;

constexpr unsigned WS_E = 0;
constexpr unsigned WS_QG = 4096;
constexpr unsigned WS_CO = WS_QG + 8 * 8 * 4 * 512 * 8;
constexpr unsigned CO_BLK = 2176;
constexpr unsigned WS_RP = WS_CO + 256 * CO_BLK;
constexpr unsigned WS_HG = WS_RP + 32 * 8 * 512 * 8;
constexpr unsigned WS_R2 = WS_HG + 8 * 256 * 8;
constexpr unsigned WS_END = WS_R2 + NMLP * 16384;
constexpr unsigned WS_ST = 6u << 20;
constexpr unsigned WS_BYTES = STAMPS ? (8u << 20) : WS_END;

constexpr unsigned L_K = 0, L_V = 65536, L_WQ = 131072, L_XN = 147456, XN_S = 1056;
constexpr unsigned L_Q = L_XN + 8 * XN_S, Q_S = 320;
constexpr unsigned L_P = L_Q + 4 * Q_S, P_S = 576;
constexpr unsigned L_RED = L_P + 4 * P_S;
constexpr unsigned L_ST = L_RED + 2048;
constexpr unsigned L_CT = L_ST + 128;
constexpr unsigned LDS_BYTES = L_CT + 256;
static_assert(LDS_BYTES <= 163840, "LDS");
constexpr unsigned L_O = L_WQ, O_S = 288;
constexpr unsigned L_RR = L_WQ + 4096, L_RR2 = L_WQ + 6144;
constexpr unsigned L_HH = L_WQ + 8192, HH_S = 96;
constexpr unsigned L_G = L_K, L_U = L_K + 32768;
enum { F_XW = 0, F_Q = 1, F_MAX = 2, F_P = 3, F_PV = 4, F_K = 5, F_VI = 6, F_V = 7  , F_TAG = 11, F_KI = 12, F_N = 13 };

struct Params {
  const void* x; const void* kc; const void* vc; const void* wq; const void* wo;
  const void* wg; const void* wu; const void* wd; void* out; void* ws;
};

__device__ __forceinline__ rsrc_t mk_rsrc(const void* p, unsigned bytes) {
  return __builtin_amdgcn_make_buffer_rsrc((void*)p, 0, bytes, 0x00020000);
}
__device__ __forceinline__ unsigned pk(float a, float b) { bf16x2 v = {(__bf16)a, (__bf16)b}; return __builtin_bit_cast(unsigned, v); }
__device__ __forceinline__ float rb(float a) { return (float)(__bf16)a; }
__device__ __forceinline__ float lo16(unsigned u) { return __uint_as_float(u << 16); }
__device__ __forceinline__ float hi16(unsigned u) { return __uint_as_float(u & 0xffff0000u); }
__device__ __forceinline__ f32x4 mfma(v4u a, v4u b, f32x4 c) {
  return __builtin_amdgcn_mfma_f32_16x16x32_bf16(__builtin_bit_cast(bf16x8, a), __builtin_bit_cast(bf16x8, b), c, 0, 0, 0);
}
template <int CTRL> __device__ __forceinline__ float dpp(float x) {
  return __builtin_bit_cast(float, __builtin_amdgcn_mov_dpp(__builtin_bit_cast(int, x), CTRL, 0xf, 0xf, true));
}
constexpr int XOR1 = 0xB1, XOR2 = 0x4E, HMIR = 0x141, MIR = 0x140, ROR4 = 0x124, ROR8 = 0x128;
__device__ __forceinline__ float x16_sum(float x) {
  auto s = __builtin_amdgcn_permlane16_swap(__float_as_uint(x), __float_as_uint(x), false, false);
  return __uint_as_float(s[0]) + __uint_as_float(s[1]);
}
__device__ __forceinline__ float x32_sum(float x) {
  auto s = __builtin_amdgcn_permlane32_swap(__float_as_uint(x), __float_as_uint(x), false, false);
  return __uint_as_float(s[0]) + __uint_as_float(s[1]);
}
__device__ __forceinline__ float x16_max(float x) {
  auto s = __builtin_amdgcn_permlane16_swap(__float_as_uint(x), __float_as_uint(x), false, false);
  return fmaxf(__uint_as_float(s[0]), __uint_as_float(s[1]));
}
__device__ __forceinline__ float x32_max(float x) {
  auto s = __builtin_amdgcn_permlane32_swap(__float_as_uint(x), __float_as_uint(x), false, false);
  return fmaxf(__uint_as_float(s[0]), __uint_as_float(s[1]));
}
__device__ __forceinline__ float wave_sum(float x) {
  x += dpp<XOR1>(x); x += dpp<XOR2>(x); x += dpp<HMIR>(x); x += dpp<MIR>(x);
  return x32_sum(x16_sum(x));
}
__device__ __forceinline__ void layernorm8(float (&v)[8]) {
  const float pv = __uint_as_float(__builtin_amdgcn_readfirstlane(__float_as_uint(v[0])));
  float d[8];
#pragma unroll
  for (int i = 0; i < 8; ++i) d[i] = v[i] - pv;
  float s = ((d[0] + d[1]) + (d[2] + d[3])) + ((d[4] + d[5]) + (d[6] + d[7]));
  float q = fmaf(d[0], d[0], d[1] * d[1]) + fmaf(d[2], d[2], d[3] * d[3]) + (fmaf(d[4], d[4], d[5] * d[5]) + fmaf(d[6], d[6], d[7] * d[7]));
  s += dpp<XOR1>(s); q += dpp<XOR1>(q);
  s += dpp<XOR2>(s); q += dpp<XOR2>(q);
  s += dpp<HMIR>(s); q += dpp<HMIR>(q);
  s += dpp<MIR>(s);  q += dpp<MIR>(q);
  s = x16_sum(s); q = x16_sum(q);
  s = x32_sum(s) * (1.0f / 512.0f); q = x32_sum(q) * (1.0f / 512.0f);
  const float var = fmaxf(fmaf(-s, s, q), 0.f);
  const float mb = rb(pv + s);
  const float den = rb(__builtin_amdgcn_sqrtf(rb(rb(var) + 1e-5f)));
  const float inv = __builtin_amdgcn_rcpf(den);
#pragma unroll
  for (int i = 0; i < 8; ++i) v[i] = rb(v[i] - mb) * inv;
}

template <int IMM, int NT>
__device__ __forceinline__ void dma16(unsigned m0v, unsigned voff, rsrc_t srd, unsigned soff) {
  if constexpr (NT)
    asm volatile("s_mov_b32 m0, %0\n\ts_nop 0\n\tbuffer_load_dwordx4 %1, %2, %3 offen offset:%4 nt lds"
                 :: "s"(m0v), "v"(voff), "s"(srd), "s"(soff), "i"(IMM) : "memory");
  else
    asm volatile("s_mov_b32 m0, %0\n\ts_nop 0\n\tbuffer_load_dwordx4 %1, %2, %3 offen offset:%4 lds"
                 :: "s"(m0v), "v"(voff), "s"(srd), "s"(soff), "i"(IMM) : "memory");
}
#define VMWAIT(n) asm volatile("s_waitcnt vmcnt(" #n ")" ::: "memory")
template <int N> __device__ __forceinline__ void vmwait() { asm volatile("s_waitcnt vmcnt(%0)" :: "i"(N) : "memory"); }
#define LGKM0() asm volatile("s_waitcnt lgkmcnt(0)" ::: "memory")
__device__ __forceinline__ void wg_barrier() { LGKM0(); __builtin_amdgcn_s_barrier(); asm volatile("" ::: "memory"); }

__device__ __forceinline__ void flag_set(unsigned ldsb, int grp, int slot) {
  const unsigned a = ldsb + L_CT + grp * 16 + slot * 4, one = 1u;
  asm volatile("s_waitcnt lgkmcnt(0)\n\ts_mov_b64 exec, 1\n\tds_write_b32 %0, %1\n\ts_mov_b64 exec, -1" :: "v"(a), "v"(one) : "memory");
}
template <int N>
__device__ __forceinline__ void flag_wait(LAS unsigned char* L, int grp) {
  for (unsigned spins = 0; spins < (1u << 22); ++spins) {
    asm volatile("" ::: "memory");
    const v4u v = *(volatile LAS v4u*)(L + L_CT + grp * 16);
    const unsigned all = N == 4 ? (v.x & v.y & v.z & v.w) : (v.x & v.y);
    if (__builtin_amdgcn_readfirstlane(all)) break;
  }
  asm volatile("" ::: "memory");
}
#define PIN(x) asm volatile("" : "+v"(x))
__device__ __forceinline__ v4u ld128(LAS unsigned char* L, unsigned off) { return *(LAS v4u*)(L + off); }
__device__ __forceinline__ v4u tr_frag(LAS unsigned char* L, unsigned off, unsigned second) {
  const s16x4 a = __builtin_amdgcn_ds_read_tr16_b64_v4i16((LAS s16x4*)(L + off));
  const s16x4 b = __builtin_amdgcn_ds_read_tr16_b64_v4i16((LAS s16x4*)(L + off + second));
  const v2u x = __builtin_bit_cast(v2u, a), y = __builtin_bit_cast(v2u, b);
  return v4u{x.x, x.y, y.x, y.y};
}
__device__ __forceinline__ v4u gld128(rsrc_t rs, unsigned off) { return __builtin_amdgcn_raw_buffer_load_b128(rs, off, 0, 16); }
__device__ __forceinline__ v2u gld64(rsrc_t rs, unsigned off) { return __builtin_amdgcn_raw_buffer_load_b64(rs, off, 0, 16); }
__device__ __forceinline__ void gst128(rsrc_t rs, unsigned off, v4u v) { __builtin_amdgcn_raw_buffer_store_b128(v, rs, off, 0, 16); }
__device__ __forceinline__ void gst64(rsrc_t rs, unsigned off, v2u v) { __builtin_amdgcn_raw_buffer_store_b64(v, rs, off, 0, 16); }

template <int N, int DLY, int SLP, int MASK>
__device__ __forceinline__ unsigned gather(rsrc_t ws, unsigned off, unsigned stride, unsigned tag, v4u (&v)[N]) {
  if (DLY) __builtin_amdgcn_s_sleep(DLY);
  unsigned spins = 0;
  if (MASK) {
    bool need[N];
#pragma unroll
    for (int k = 0; k < N; ++k) need[k] = true;
    for (;; ++spins) {
      asm volatile("" ::: "memory");
#pragma unroll
      for (int k = 0; k < N; ++k) if (need[k]) v[k] = gld128(ws, off + k * stride);
      bool any = false;
#pragma unroll
      for (int k = 0; k < N; ++k) { if (need[k]) need[k] = !__all(v[k].y == tag && v[k].w == tag); any = any || need[k]; }
      if (!any || spins >= SPIN_CAP) break;
      if (SLP) __builtin_amdgcn_s_sleep(SLP);
    }
  } else {
    for (;; ++spins) {
      asm volatile("" ::: "memory");
#pragma unroll
      for (int k = 0; k < N; ++k) v[k] = gld128(ws, off + k * stride);
      bool ok = true;
#pragma unroll
      for (int k = 0; k < N; ++k) ok = ok && v[k].y == tag && v[k].w == tag;
      if (__all(ok) || spins >= SPIN_CAP) break;
      if (SLP) __builtin_amdgcn_s_sleep(SLP);
    }
  }
  return spins + 1;
}

#define STAMP(i) do {} while (0)
#define COUNT(i, v) do {} while (0)

__global__ void __launch_bounds__(512, 2) fused_decode(Params p) {
  __shared__ __attribute__((aligned(16))) unsigned char lds_raw[LDS_BYTES];
  LAS unsigned char* L = (LAS unsigned char*)lds_raw;
  const unsigned ldsb = (unsigned)(size_t)L;
  STAMP(0);
  u64 code_base; asm volatile("s_getpc_b64 %0" : "=s"(code_base));
  code_base &= ~(u64)255;
  u64 t_begin; asm volatile("s_memrealtime %0\n\ts_waitcnt lgkmcnt(0)" : "=s"(t_begin) :: "memory");
  asm volatile("" :: "s"(p.x), "s"(p.kc), "s"(p.vc), "s"(p.wq), "s"(p.wo), "s"(p.wg), "s"(p.wu), "s"(p.wd), "s"(p.out), "s"(p.ws));
  const int tid = threadIdx.x, lane = tid & 63;
  const int w = __builtin_amdgcn_readfirstlane(tid >> 6);
  const int c = blockIdx.x, hkv = c & 7, j = c >> 3, b = j >> 2, s = j & 3;
  const int g = lane >> 4, i16 = lane & 15;
  const rsrc_t ws = mk_rsrc(p.ws, WS_BYTES);
  unsigned tag = 0;
  const bool roleA = j < 16;
  const int hi4 = hkv >> 2;
  const bool roleB = j >= 16 && j - 16 < 8 - 2 * hi4;
  const int m = (8 - 2 * hi4) * hkv + 8 * hi4 + (j - 16);
  const int hl = j >> 2, cb128 = j & 3, head = 4 * hkv + hl;
  const unsigned coblk = WS_CO + (unsigned)(hkv * 32 + j) * CO_BLK;
  const int rot = (j + 3 * hkv) & 7;

  if (w >= 6) {
    if (!(roleA || roleB)) {
      if (j == 29) {
        typedef __attribute__((address_space(1))) const unsigned gcu;
        unsigned acc = 0;
#pragma unroll
        for (int q = 0; q < 2; ++q) {
          const unsigned o = (unsigned)((w - 6) * 2 + q) * 8192u + lane * 128u;
          if (o < 20480u) acc += *(gcu*)(code_base + o);
        }
        asm volatile("s_waitcnt vmcnt(0)" :: "v"(acc) : "memory");
      }
      return;
    }
    const int l = w - 6;
    __builtin_amdgcn_s_barrier();
    asm volatile("" ::: "memory");
    if (roleA) {
      const rsrc_t ro = mk_rsrc((const char*)p.wo + (size_t)head * 128 * 1024 + cb128 * 256, 128 * 1024 - cb128 * 256);
      unsigned vo[2];
#pragma unroll
      for (int r = 0; r < 2; ++r) vo[r] = g * 1024 + ((i16 ^ (((4 * r + g) & 7) << 1)) * 16);
      flag_wait<2>(L, F_VI);
      flag_wait<4>(L, F_MAX);
      STAMP(1);
#pragma unroll
      for (int q = 0; q < 16; ++q) dma16<0, NT_W>(ldsb + L_K + (64 * l + 4 * q) * 256, vo[q & 1], ro, (64 * l + 4 * q) * 1024);
      STAMP(2);
      VMWAIT(0);
    } else {
      const rsrc_t rg = mk_rsrc((const char*)(l == 0 ? p.wg : p.wu) + m * 64, 512 * FF * 2 - m * 64);
      const unsigned vg = (lane >> 2) * (FF * 2) + (((lane & 3) ^ (2 * ((lane >> 4) & 1) + l)) * 16);
      const rsrc_t rd = mk_rsrc((const char*)p.wd + (size_t)m * 32 * 1024, 32 * 1024);
      flag_wait<2>(L, F_VI);
      flag_wait<4>(L, F_MAX);
      STAMP(1);
#pragma unroll
      for (int q = 0; q < 32; ++q) {
        const unsigned qq = (q + 5 * m) & 31;
        dma16<0, NT_W>(ldsb + L_G + l * 32768 + qq * 1024, vg, rg, qq * 16 * (FF * 2));
      }
      STAMP(2);
      flag_wait<4>(L, F_PV);
#pragma unroll
      for (int q = 0; q < 16; ++q) dma16<0, NT_W>(ldsb + L_V + (16 * l + q) * 1024, (unsigned)(lane ^ ((q & 7) << 1)) * 16, rd, (16 * l + q) * 1024);
      STAMP(3);
      VMWAIT(0);
    }
    STAMP(4);
  } else if (w >= 4) {
    const int l = w - 4;
    const size_t win = ((size_t)(b * NHKV + hkv) * SK + (SK - WIN) + 256 * s) * (DH * 2);
    const rsrc_t rk = mk_rsrc((const char*)p.kc + win, 65536), rv = mk_rsrc((const char*)p.vc + win, 65536);
    unsigned vk[4], vv[2];
#pragma unroll
    for (int r = 0; r < 4; ++r) vk[r] = g * 256 + ((i16 ^ (4 * r + g)) * 16);
#pragma unroll
    for (int r = 0; r < 2; ++r) vv[r] = g * 256 + ((i16 ^ (((4 * r + g) & 7) << 1)) * 16);
    asm volatile("s_nop 4" ::: "memory");
#define WIN_WAIT() vmwait<KV_WIN - 1>()
#define GOFF(a) ((unsigned)(32 * (((a) + rot) & 7) + 16 * l) * 256)
#define K_ONE(a, r) dma16<1024 * (r), NT_KV>(ldsb + L_K + GOFF(a), vk[r], rk, GOFF(a))
#define V_ONE(a, r) dma16<1024 * (r), NT_KV>(ldsb + L_V + GOFF(a), vv[(r) & 1], rv, GOFF(a))
#define K_GROUP(a) { WIN_WAIT(); K_ONE(a, 0); WIN_WAIT(); K_ONE(a, 1); WIN_WAIT(); K_ONE(a, 2); WIN_WAIT(); K_ONE(a, 3); }
#define V_GROUP(a) { WIN_WAIT(); V_ONE(a, 0); WIN_WAIT(); V_ONE(a, 1); WIN_WAIT(); V_ONE(a, 2); WIN_WAIT(); V_ONE(a, 3); }
    K_ONE(0, 0); K_ONE(0, 1); K_ONE(0, 2); K_ONE(0, 3);
    __builtin_amdgcn_s_barrier();
    asm volatile("" ::: "memory");
    VMWAIT(3);
    K_GROUP(1) K_GROUP(2) K_GROUP(3) K_GROUP(4) K_GROUP(5) K_GROUP(6) K_GROUP(7)
    flag_set(ldsb, F_KI, l);
    STAMP(1);
    V_GROUP(0) V_GROUP(1) V_GROUP(2)
    vmwait<(KV_WIN < 12 ? KV_WIN : 12)>();
    flag_set(ldsb, F_K, l);
    STAMP(2);
    V_GROUP(3) V_GROUP(4) V_GROUP(5) V_GROUP(6) V_GROUP(7)
    flag_set(ldsb, F_VI, l);
    STAMP(3);
    if (KV_WIN > 24) { VMWAIT(24); }
    flag_set(ldsb, F_V + 0, l);
    if (KV_WIN > 16) { VMWAIT(16); }
    flag_set(ldsb, F_V + 1, l);
    VMWAIT(8);  flag_set(ldsb, F_V + 2, l);
    VMWAIT(0);  flag_set(ldsb, F_V + 3, l);
    STAMP(4);
  } else {
    const int qcb = j & 7, kq = j >> 3;
    {
      const rsrc_t rx = mk_rsrc(p.x, NB * DM * 2);
      const int n0 = hkv * 512 + 64 * qcb;
      const rsrc_t rq = mk_rsrc((const char*)p.wq + (size_t)kq * 128 * 8192 + n0 * 2, 128 * 8192 - n0 * 2);
      const unsigned vq = (lane >> 3) * 8192 + (((lane & 7) ^ (((lane >> 4) & 3) << 1)) * 16);
      asm volatile("s_nop 4\n\tbuffer_load_dword %0, off, %1, 0 sc1" : "=&v"(tag) : "s"(ws) : "memory");
      dma16<0, 0>(ldsb + L_XN + (2 * w) * XN_S, lane * 16, rx, (2 * w) * 1024);
      dma16<0, 0>(ldsb + L_XN + (2 * w + 1) * XN_S, lane * 16, rx, (2 * w + 1) * 1024);
#pragma unroll
      for (int q = 0; q < 4; ++q) dma16<0, NT_W>(ldsb + L_WQ + (32 * w + 8 * q) * 128, vq, rq, (32 * w + 8 * q) * 8192);
    }
    if (w == 0 && lane < 4 * F_N) ((LAS unsigned*)(L + L_CT))[lane] = 0u;
    wg_barrier();
    asm volatile("s_waitcnt vmcnt(4)" : "+v"(tag) :: "memory");
    STAMP(1);
    tag += 1u;
    if (w == 0 && lane == 0) ((LAS unsigned*)(L + L_CT))[4 * F_TAG] = tag;
    {
      const v4u raw0 = ld128(L, L_XN + (2 * w) * XN_S + lane * 16), raw1 = ld128(L, L_XN + (2 * w + 1) * XN_S + lane * 16);
      float v0[8] = {lo16(raw0.x), hi16(raw0.x), lo16(raw0.y), hi16(raw0.y), lo16(raw0.z), hi16(raw0.z), lo16(raw0.w), hi16(raw0.w)};
      float v1[8] = {lo16(raw1.x), hi16(raw1.x), lo16(raw1.y), hi16(raw1.y), lo16(raw1.z), hi16(raw1.z), lo16(raw1.w), hi16(raw1.w)};
      layernorm8(v0); layernorm8(v1);
      const int t = lane >> 2, q = lane & 3;
      const unsigned base = L_XN + (2 * w) * XN_S + (32 * t + 16 * (q & 1) + 4 * (q >> 1)) * 2;
      asm volatile("" ::: "memory");
      *(LAS v2u*)(L + base) = v2u{pk(v0[0], v0[1]), pk(v0[2], v0[3])};
      *(LAS v2u*)(L + base + 16) = v2u{pk(v0[4], v0[5]), pk(v0[6], v0[7])};
      *(LAS v2u*)(L + base + XN_S) = v2u{pk(v1[0], v1[1]), pk(v1[2], v1[3])};
      *(LAS v2u*)(L + base + XN_S + 16) = v2u{pk(v1[4], v1[5]), pk(v1[6], v1[7])};
    }
    VMWAIT(0);
    STAMP(2);
    flag_set(ldsb, F_XW, w);
    flag_wait<4>(L, F_XW);
    const int rr = 4 * g + (i16 >> 2), pp = i16 & 3;
    {
      const unsigned ab = L_WQ + rr * 128 + (((2 * w + (pp >> 1)) ^ (((rr >> 1) & 3) << 1)) * 16) + 8 * (pp & 1);
      const unsigned bb = L_XN + (i16 & 7) * XN_S + (128 * kq + 8 * g) * 2;
      v4u a[4], bq[4];
#pragma unroll
      for (int t = 0; t < 4; ++t) { a[t] = tr_frag(L, ab + t * 4096, 2048); bq[t] = ld128(L, bb + t * 64); }
      f32x4 acc = {0.f, 0.f, 0.f, 0.f};
#pragma unroll
      for (int t = 0; t < 4; ++t) acc = mfma(bq[t], a[t], acc);
      const int b1 = 4 * (g & 1) + (g >> 1);
      const unsigned qa = WS_QG + (unsigned)(((hkv * 8 + b1) * 4 + kq) * 512 + 64 * qcb + 16 * w + i16) * 8;
      gst64(ws, qa, v2u{__float_as_uint(g < 2 ? acc[0] : acc[1]), tag});
      gst64(ws, qa + 2 * 4 * 512 * 8, v2u{__float_as_uint(g < 2 ? acc[2] : acc[3]), tag});
    }
    STAMP(3);
    {
      const unsigned off = WS_QG + (unsigned)((hkv * 8 + b) * 4 * 512 + 128 * w + 2 * lane) * 8;
      v4u v[4];
      flag_wait<2>(L, F_KI);
      { const unsigned n = gather<4, Q_DLY, Q_SLP, Q_MASK>(ws, off, 4096, tag, v); COUNT(16, n); }
      const float q0 = (__uint_as_float(v[0].x) + __uint_as_float(v[1].x)) + (__uint_as_float(v[2].x) + __uint_as_float(v[3].x));
      const float q1 = (__uint_as_float(v[0].z) + __uint_as_float(v[1].z)) + (__uint_as_float(v[2].z) + __uint_as_float(v[3].z));
      *(LAS unsigned*)(L + L_Q + w * Q_S + lane * 4) = pk(q0, q1);
    }
    flag_set(ldsb, F_Q, w);
    flag_wait<4>(L, F_Q);
    STAMP(4);
    float sc[4];
    const int hd = i16 & 3, dup = i16 >> 2;
    {
      v4u qf[4];
#pragma unroll
      for (int t = 0; t < 4; ++t) qf[t] = ld128(L, L_Q + hd * Q_S + (4 * t + g) * 16);
      flag_wait<2>(L, F_K);
      STAMP(5);
      v4u kf[16];
#pragma unroll
      for (int T = 0; T < 4; ++T)
#pragma unroll
        for (int t = 0; t < 4; ++t) kf[4 * T + t] = ld128(L, L_K + (64 * w + 16 * T + i16) * 256 + (((4 * t + g) ^ i16) * 16));
      __builtin_amdgcn_sched_barrier(0);
      f32x4 acc[4];
#pragma unroll
      for (int T = 0; T < 4; ++T) acc[T] = f32x4{0.f, 0.f, 0.f, 0.f};
#pragma unroll
      for (int t = 0; t < 4; ++t)
#pragma unroll
        for (int T = 0; T < 4; ++T) acc[T] = mfma(kf[4 * T + t], qf[t], acc[T]);
#pragma unroll
      for (int r = 0; r < 4; ++r) {
        const float a01 = (dup & 1) ? acc[1][r] : acc[0][r], a23 = (dup & 1) ? acc[3][r] : acc[2][r];
        sc[r] = rb(rb((dup & 2) ? a23 : a01) * SCALE);
      }
    }
    float mx, inv, lse;
    {
      float v = fmaxf(fmaxf(sc[0], sc[1]), fmaxf(sc[2], sc[3]));
      v = fmaxf(v, dpp<ROR4>(v)); v = fmaxf(v, dpp<ROR8>(v));
      v = x32_max(x16_max(v));
      if (lane < 4) ((LAS float*)(L + L_ST))[lane * 4 + w] = v;
      flag_set(ldsb, F_MAX, w);
      flag_wait<4>(L, F_MAX);
      const f32x4 m4 = *(LAS f32x4*)(L + L_ST + hd * 16);
      mx = fmaxf(fmaxf(m4[0], m4[1]), fmaxf(m4[2], m4[3]));
      float e[4];
#pragma unroll
      for (int r = 0; r < 4; ++r) e[r] = __builtin_amdgcn_exp2f((sc[r] - mx) * LOG2E);
      *(LAS v2u*)(L + L_P + hd * P_S + (32 * (2 * w + (dup >> 1)) + 8 * g + 4 * (dup & 1)) * 2) = v2u{pk(e[0], e[1]), pk(e[2], e[3])};
      float t = (e[0] + e[1]) + (e[2] + e[3]);
      t += dpp<ROR4>(t); t += dpp<ROR8>(t);
      t = x32_sum(x16_sum(t));
      if (lane < 4) ((LAS float*)(L + L_ST))[16 + lane * 4 + w] = t;
      flag_set(ldsb, F_P, w);
      flag_wait<4>(L, F_P);
      const f32x4 l4 = *(LAS f32x4*)(L + L_ST + 64 + hd * 16), l4g = *(LAS f32x4*)(L + L_ST + 64 + g * 16);
      inv = __builtin_amdgcn_rcpf((l4g[0] + l4g[1]) + (l4g[2] + l4g[3]));
      lse = fmaf(mx, LOG2E, __builtin_amdgcn_logf((l4[0] + l4[1]) + (l4[2] + l4[3])));
    }
    STAMP(6);
    {
      v4u pf[8];
#pragma unroll
      for (int u = 0; u < 8; ++u) pf[u] = ld128(L, L_P + dup * P_S + (32 * ((u + rot) & 7) + 8 * g) * 2);
      unsigned vb0 = L_V + rr * 256 + (((4 * w + (pp >> 1)) ^ ((rr & 7) << 1)) * 16) + 8 * (pp & 1);
      unsigned vb1 = L_V + rr * 256 + (((4 * w + 2 + (pp >> 1)) ^ ((rr & 7) << 1)) * 16) + 8 * (pp & 1);
      unsigned oaddr = coblk + g * 512 + w * 128 + i16 * 8;
      PIN(vb0); PIN(vb1); PIN(oaddr); PIN(inv); PIN(lse);
      if (w == 0 && lane < 4) gst64(ws, coblk + 2048 + lane * 8, v2u{__float_as_uint(lse), tag});
      f32x4 o0 = {0.f, 0.f, 0.f, 0.f}, o1 = {0.f, 0.f, 0.f, 0.f};
#pragma unroll
      for (int cc = 0; cc < 4; ++cc) {
        const unsigned ua = ((2 * cc + rot) & 7) * 8192, ub = ((2 * cc + 1 + rot) & 7) * 8192;
        unsigned a0 = vb0 + ua, a1 = vb1 + ua, b0 = vb0 + ub, b1 = vb1 + ub;
        PIN(a0); PIN(a1); PIN(b0); PIN(b1);
        flag_wait<2>(L, F_V + cc);
        const v4u a00 = tr_frag(L, a0, 4096), a10 = tr_frag(L, a1, 4096);
        const v4u a01 = tr_frag(L, b0, 4096), a11 = tr_frag(L, b1, 4096);
        __builtin_amdgcn_sched_barrier(0);
        o0 = mfma(pf[2 * cc], a00, o0); o1 = mfma(pf[2 * cc], a10, o1);
        o0 = mfma(pf[2 * cc + 1], a01, o0); o1 = mfma(pf[2 * cc + 1], a11, o1);
      }
      STAMP(7);
      if (roleB) flag_set(ldsb, F_PV, w);
      const float va = o0[0] * inv, vb = o1[0] * inv;
      const float na = dpp<XOR1>(va), nb = dpp<XOR1>(vb);
      gst64(ws, oaddr, v2u{(lane & 1) ? pk(nb, vb) : pk(va, na), tag});
    }
    STAMP(8);
  }

  if (roleA) {
    wg_barrier();
    tag = ((LAS unsigned*)(L + L_CT))[4 * F_TAG];
    STAMP(9);
    const int rr = 4 * g + (i16 >> 2), pp = i16 & 3;
    v4u af[4];
    {
      const unsigned ab = L_K + rr * 256 + (((2 * w + (pp >> 1)) ^ ((rr & 7) << 1)) * 16) + 8 * (pp & 1);
#pragma unroll
      for (int t = 0; t < 4; ++t) af[t] = tr_frag(L, ab + t * 8192, 4096);
    }
    const int br = j & 7, cb32 = (j >> 3) * 8 + hkv;
    const unsigned xr = ((const unsigned*)p.x)[br * 256 + cb32 * 16 + (w & 1) * 8 + (lane & 7)];
    unsigned rpaddr = WS_RP + (unsigned)((head * 8 + 4 * (g & 1) + (g >> 1)) * 512 + cb128 * 128 + 16 * w + i16) * 8;
    unsigned obase = L_O + (i16 & 7) * O_S + 8 * g * 2;
    PIN(rpaddr); PIN(obase);
    {
      const unsigned pb = WS_CO + (unsigned)(hkv * 32 + 4 * w) * CO_BLK;
      const unsigned o0 = pb + (lane >> 5) * CO_BLK + hl * 512 + (lane & 31) * 16, o1 = o0 + 2 * CO_BLK;
      const unsigned os = pb + (lane & 3) * CO_BLK + 2048 + hl * 8;
      const int ch = lane & 31;
      unsigned owr = L_O + w * O_S + (32 * (ch >> 3) + 8 * ((ch & 7) >> 1) + 2 * (ch & 1)) * 2;
      PIN(owr);
      v4u v0, v1; v2u st;
      if (CO_DLY) __builtin_amdgcn_s_sleep(CO_DLY);
      unsigned npass = 0;
      for (unsigned spins = 0;; ++spins) {
        asm volatile("" ::: "memory");
        v0 = gld128(ws, o0); v1 = gld128(ws, o1); st = gld64(ws, os);
        ++npass;
        if (__all(v0.y == tag && v0.w == tag && v1.y == tag && v1.w == tag && st.y == tag) || spins >= SPIN_CAP) break;
        if (CO_SLP) __builtin_amdgcn_s_sleep(CO_SLP);
      }
      COUNT(17, npass);
      STAMP(10);
      const float Ls = __uint_as_float(st.x);
      float Lm = fmaxf(Ls, dpp<XOR1>(Ls)); Lm = fmaxf(Lm, dpp<XOR2>(Lm));
      const float e = __builtin_amdgcn_exp2f(Ls - Lm);
      float es = e + dpp<XOR1>(e); es += dpp<XOR2>(es);
      const float wt = e * __builtin_amdgcn_rcpf(es);
      const float w0 = dpp<0x00>(wt), w1 = dpp<0x55>(wt), w2 = dpp<0xAA>(wt), w3 = dpp<0xFF>(wt);
      const float wa = lane < 32 ? w0 : w1, wb = lane < 32 ? w2 : w3;
      const float a0 = x32_sum(fmaf(wa, lo16(v0.x), wb * lo16(v1.x))), a1 = x32_sum(fmaf(wa, hi16(v0.x), wb * hi16(v1.x)));
      const float a2 = x32_sum(fmaf(wa, lo16(v0.z), wb * lo16(v1.z))), a3 = x32_sum(fmaf(wa, hi16(v0.z), wb * hi16(v1.z)));
      if (lane < 32) { *(LAS unsigned*)(L + owr) = pk(a0, a1); *(LAS unsigned*)(L + owr + 8) = pk(a2, a3); }
    }
    wg_barrier();
    {
      v4u bo[4];
#pragma unroll
      for (int t = 0; t < 4; ++t) bo[t] = ld128(L, obase + t * 64);
      f32x4 acc = {0.f, 0.f, 0.f, 0.f};
#pragma unroll
      for (int t = 0; t < 4; ++t) acc = mfma(bo[t], af[t], acc);
      gst64(ws, rpaddr, v2u{__float_as_uint(g < 2 ? acc[0] : acc[1]), tag});
      gst64(ws, rpaddr + 2 * 512 * 8, v2u{__float_as_uint(g < 2 ? acc[2] : acc[3]), tag});
    }
    STAMP(11);
    if (w < 2) {
      const int grp = lane >> 3, pr = lane & 7;
      float h0, h1;
      {
        const unsigned off = WS_RP + (unsigned)((grp * 8 + br) * 512 + 32 * cb32 + 16 * w + 2 * pr) * 8;
        v4u v[4];
        { const unsigned n = gather<4, R_DLY, R_SLP, R_MASK>(ws, off, 8u * 8 * 512 * 8, tag, v); COUNT(18, n); }
        STAMP(12);
        float s0 = (__uint_as_float(v[0].x) + __uint_as_float(v[1].x)) + (__uint_as_float(v[2].x) + __uint_as_float(v[3].x));
        float s1 = (__uint_as_float(v[0].z) + __uint_as_float(v[1].z)) + (__uint_as_float(v[2].z) + __uint_as_float(v[3].z));
        s0 += dpp<ROR8>(s0); s1 += dpp<ROR8>(s1);
        s0 = x32_sum(x16_sum(s0)); s1 = x32_sum(x16_sum(s1));
        h0 = rb(lo16(xr) + rb(s0)); h1 = rb(hi16(xr) + rb(s1));
        if (lane < 8) gst64(ws, WS_HG + (unsigned)(br * 256 + 16 * cb32 + 8 * w + lane) * 8, v2u{pk(h0, h1), tag});
      }
      STAMP(13);
    } else if (w == 2) {
      const int a = hkv * 16 + j, grp = lane >> 3, bb8 = lane & 7;
      v4u hv[1];
      gather<1, 8, 4, 0>(ws, WS_HG + (unsigned)(bb8 * 256 + 2 * a) * 8, 0, tag, hv);
      const unsigned off = WS_R2 + (unsigned)grp * 16384 + (a >> 3) * 1024 + (a & 3) * 256 + ((a >> 2) & 1) * 128 + bb8 * 16;
      v4u v[7];
      { const unsigned n = gather<7, R2_DLY, R2_SLP, R2_MASK>(ws, off, 8u * 16384, tag, v); COUNT(19, n); }
      STAMP(14);
      float s0 = lo16(v[0].x), s1 = hi16(v[0].x), s2 = lo16(v[0].z), s3 = hi16(v[0].z);
#pragma unroll
      for (int k = 1; k < 7; ++k) { s0 += lo16(v[k].x); s1 += hi16(v[k].x); s2 += lo16(v[k].z); s3 += hi16(v[k].z); }
      s0 += dpp<ROR8>(s0); s1 += dpp<ROR8>(s1); s2 += dpp<ROR8>(s2); s3 += dpp<ROR8>(s3);
      s0 = x32_sum(x16_sum(s0)); s1 = x32_sum(x16_sum(s1)); s2 = x32_sum(x16_sum(s2)); s3 = x32_sum(x16_sum(s3));
      if (lane < 8)
        ((v2u*)p.out)[bb8 * 128 + a] = v2u{pk(lo16(hv[0].x) + rb(s0), hi16(hv[0].x) + rb(s1)), pk(lo16(hv[0].z) + rb(s2), hi16(hv[0].z) + rb(s3))};
      if (c == 0 && lane == 0) __builtin_amdgcn_raw_buffer_store_b32(tag, ws, WS_E, 0, 16);
      STAMP(15);
    }
  } else if (roleB) {
    wg_barrier();
    tag = ((LAS unsigned*)(L + L_CT))[4 * F_TAG];
    STAMP(9);
    const int rr = 4 * g + (i16 >> 2), pp = i16 & 3;
    v4u df[4], gf[16];
    {
#pragma unroll
      for (int x = 0; x < 4; ++x)
        df[x] = tr_frag(L, L_V + rr * 1024 + (((2 * (4 * w + x) + (pp >> 1)) ^ ((rr & 7) << 1)) * 16) + 8 * (pp & 1), 16 * 1024);
      if (w < 4) {
        const int mat = pp >> 1;
        const unsigned gb = L_G + mat * 32768 + rr * 64 + ((w ^ (2 * ((rr >> 2) & 1) + mat)) * 16) + 8 * (pp & 1);
#pragma unroll
        for (int t = 0; t < 16; ++t) gf[t] = tr_frag(L, gb + t * 2048, 1024);
      }
    }
    unsigned r2addr = WS_R2 + (unsigned)m * 16384 + 2 * w * 1024 + lane * 16;
    unsigned bb = L_XN + (i16 & 7) * XN_S + 8 * g * 2;
    unsigned hnw = L_XN + w * XN_S + (32 * (lane >> 3) + 8 * (lane & 3) + 4 * ((lane >> 2) & 1)) * 2;
    unsigned hhr = L_HH + (i16 & 7) * HH_S + g * 16;
    unsigned hhw = L_HH + i16 * HH_S + (8 * ((2 * w + g) & 3) + 4 * ((2 * w + g) >> 2)) * 2;
    PIN(r2addr); PIN(bb); PIN(hnw); PIN(hhr); PIN(hhw);
    {
      const unsigned off = WS_HG + (unsigned)(w * 256 + 2 * lane) * 8;
      v4u hv[2];
      { const unsigned n = gather<2, H_DLY, H_SLP, H_MASK>(ws, off, 1024, tag, hv); COUNT(20, n); }
      const v4u v0 = hv[0], v1 = hv[1];
      STAMP(10);
      float v[8] = {lo16(v0.x), hi16(v0.x), lo16(v0.z), hi16(v0.z), lo16(v1.x), hi16(v1.x), lo16(v1.z), hi16(v1.z)};
      layernorm8(v);
      *(LAS v2u*)(L + hnw) = v2u{pk(v[0], v[1]), pk(v[2], v[3])};
      *(LAS v2u*)(L + hnw + 512) = v2u{pk(v[4], v[5]), pk(v[6], v[7])};
    }
    wg_barrier();
    STAMP(11);
    if (w < 4) {
      v4u hb[16];
#pragma unroll
      for (int t = 0; t < 16; ++t) hb[t] = ld128(L, bb + t * 64);
      __builtin_amdgcn_sched_barrier(0);
      f32x4 acc = {0.f, 0.f, 0.f, 0.f}, acc2 = {0.f, 0.f, 0.f, 0.f};
#pragma unroll
      for (int t = 0; t < 16; t += 2) { acc = mfma(gf[t], hb[t], acc); acc2 = mfma(gf[t + 1], hb[t + 1], acc2); }
      float hh[4];
#pragma unroll
      for (int r = 0; r < 4; ++r) {
        const float v = rb(acc[r] + acc2[r]);
        auto sw = __builtin_amdgcn_permlane32_swap(__float_as_uint(v), __float_as_uint(v), false, false);
        const float gt = __uint_as_float(sw[0]), up = __uint_as_float(sw[1]);
        const float sg = rb(__builtin_amdgcn_rcpf(1.0f + __builtin_amdgcn_exp2f(-gt * LOG2E)));
        hh[r] = sg * up;
      }
      if (lane < 32 && i16 < 8) *(LAS v2u*)(L + hhw) = v2u{pk(hh[0], hh[1]), pk(hh[2], hh[3])};
    }
    wg_barrier();
    STAMP(12);
    {
      const v4u bf = ld128(L, hhr);
      const int half = i16 >> 3;
      f32x4 acc[4];
#pragma unroll
      for (int x = 0; x < 4; ++x) acc[x] = mfma(df[x], bf, f32x4{0.f, 0.f, 0.f, 0.f});
      STAMP(14);
#pragma unroll
      for (int y = 0; y < 2; ++y) {
        const f32x4 e = acc[2 * y], d = acc[2 * y + 1];
        const float o0 = half ? d[0] : e[0], o1 = half ? d[1] : e[1], o2 = half ? d[2] : e[2], o3 = half ? d[3] : e[3];
        gst128(ws, r2addr + y * 1024, v4u{pk(o0, o1), tag, pk(o2, o3), tag});
      }
    }
    STAMP(13);
  }
}
}

__global__ void k_stamp(unsigned long long* p) { unsigned long long t; asm volatile("s_memrealtime %0\n\ts_waitcnt lgkmcnt(0)" : "=s"(t) :: "memory"); if (threadIdx.x == 0) __hip_atomic_store(p, t, __ATOMIC_RELAXED, __HIP_MEMORY_SCOPE_AGENT); }
__global__ void k_padto(const unsigned long long* p) { const unsigned long long t0 = __hip_atomic_load(p, __ATOMIC_RELAXED, __HIP_MEMORY_SCOPE_AGENT);
  for (unsigned n = 0; n < 100000; ++n) { unsigned long long t; asm volatile("s_memrealtime %0\n\ts_waitcnt lgkmcnt(0)" : "=s"(t) :: "memory"); if (t - t0 >= 3000) break; } }
extern "C" void kernel_launch(void* const* d_in, const int* in_sizes, int n_in, void* d_out, int out_size,
                              void* d_ws, size_t ws_size, hipStream_t stream) {
  fk::Params p;
  p.x = d_in[0]; p.kc = d_in[1]; p.vc = d_in[2]; p.wq = d_in[3]; p.wo = d_in[4];
  p.wg = d_in[6]; p.wu = d_in[7]; p.wd = d_in[8]; p.out = d_out; p.ws = d_ws;
  hipLaunchKernelGGL(k_stamp, dim3(1), dim3(64), 0, stream, (unsigned long long*)((char*)d_ws + (5u << 20)));
  hipLaunchKernelGGL(fk::fused_decode, dim3(256), dim3(512), 0, stream, p);
  hipLaunchKernelGGL(k_padto, dim3(1), dim3(64), 0, stream, (const unsigned long long*)((char*)d_ws + (5u << 20)));
}
```

```cpp
#include <hip/hip_runtime.h>
#include <hip/hip_bf16.h>
#include <stdint.h>

#define STAMPS 0
#define NT_KV 1
#define NT_W 1
#define KV_WIN 12
#define CODE_BYTES 14080
#define CO_DLY 6
#define CO_SLP 0
#define R_DLY 20
#define R_SLP 0
#define H_SLP 4
#define R2_SLP 4
#define RW_START 9
#define Q_TRIG 2
#define RWA_START RW_START
#define K_HOLD 0
#define MASKED 0
#define ACK_CO 0
#define PAD_T 0

namespace fk {
#define LAS __attribute__((address_space(3)))
typedef unsigned v4u __attribute__((ext_vector_type(4)));
typedef unsigned v2u __attribute__((ext_vector_type(2)));
typedef float f32x4 __attribute__((ext_vector_type(4)));
typedef short s16x4 __attribute__((ext_vector_type(4)));
typedef __bf16 bf16x8 __attribute__((ext_vector_type(8)));
typedef __bf16 bf16x2 __attribute__((ext_vector_type(2)));
typedef unsigned long long u64;
typedef __amdgpu_buffer_rsrc_t rsrc_t;

constexpr int NB = 8, DM = 512, NHKV = 8, SK = 4096, WIN = 1024, DH = 128, FF = 1792;
constexpr int NMLP = 56;
constexpr float SCALE = 0.08838834764831845f;
constexpr float LOG2E = 1.4426950408889634f;
constexpr unsigned SPIN_CAP = 1u << 15;

constexpr unsigned WS_E = 0;
constexpr unsigned WS_QG = 4096;
constexpr unsigned WS_CO = WS_QG + 8 * 8 * 4 * 512 * 8;
constexpr unsigned CO_BLK = 2176;
constexpr unsigned WS_RP = WS_CO + 256 * CO_BLK;
constexpr unsigned WS_HG = WS_RP + 32 * 8 * 512 * 8;
constexpr unsigned WS_R2 = WS_HG + 8 * 256 * 8;
constexpr unsigned WS_END = WS_R2 + NMLP * 16384;
constexpr unsigned WS_ST = 6u << 20;
constexpr unsigned WS_BYTES = STAMPS ? (8u << 20) : WS_END;

constexpr unsigned L_K = 0, L_V = 65536, L_WQ = 131072, L_XN = 147456, XN_S = 1056;
constexpr unsigned L_Q = L_XN + 8 * XN_S, Q_S = 320;
constexpr unsigned L_P = L_Q + 4 * Q_S, P_S = 576;
constexpr unsigned L_JUNK = L_P + 4 * P_S;
constexpr unsigned L_ST = L_JUNK + 256;
constexpr unsigned L_CT = L_ST + 128;
constexpr unsigned LDS_BYTES = L_CT + 256;
static_assert(LDS_BYTES <= 163840, "LDS");
constexpr unsigned L_O = L_WQ, O_S = 288;
constexpr unsigned L_HH = L_WQ + 8192, HH_S = 96;
constexpr unsigned L_G = L_K;
enum { F_XW = 0, F_Q = 1, F_MAX = 2, F_P = 3, F_PV = 4, F_K = 5, F_VI = 6, F_V = 7  , F_TAG = 11, F_KI = 12, F_N = 13 };

struct Params {
  const void* x; const void* kc; const void* vc; const void* wq; const void* wo;
  const void* wg; const void* wu; const void* wd; void* out; void* ws;
};

__device__ __forceinline__ rsrc_t mk_rsrc(const void* p, unsigned bytes) {
  return __builtin_amdgcn_make_buffer_rsrc((void*)p, 0, bytes, 0x00020000);
}
__device__ __forceinline__ unsigned pk(float a, float b) { bf16x2 v = {(__bf16)a, (__bf16)b}; return __builtin_bit_cast(unsigned, v); }
__device__ __forceinline__ float rb(float a) { return (float)(__bf16)a; }
__device__ __forceinline__ float lo16(unsigned u) { return __uint_as_float(u << 16); }
__device__ __forceinline__ float hi16(unsigned u) { return __uint_as_float(u & 0xffff0000u); }
__device__ __forceinline__ f32x4 mfma(v4u a, v4u b, f32x4 c) {
  return __builtin_amdgcn_mfma_f32_16x16x32_bf16(__builtin_bit_cast(bf16x8, a), __builtin_bit_cast(bf16x8, b), c, 0, 0, 0);
}
template <int CTRL> __device__ __forceinline__ float dpp(float x) {
  return __builtin_bit_cast(float, __builtin_amdgcn_mov_dpp(__builtin_bit_cast(int, x), CTRL, 0xf, 0xf, true));
}
constexpr int XOR1 = 0xB1, XOR2 = 0x4E, HMIR = 0x141, MIR = 0x140, ROR4 = 0x124, ROR8 = 0x128;
__device__ __forceinline__ float x16_sum(float x) {
  auto s = __builtin_amdgcn_permlane16_swap(__float_as_uint(x), __float_as_uint(x), false, false);
  return __uint_as_float(s[0]) + __uint_as_float(s[1]);
}
__device__ __forceinline__ float x32_sum(float x) {
  auto s = __builtin_amdgcn_permlane32_swap(__float_as_uint(x), __float_as_uint(x), false, false);
  return __uint_as_float(s[0]) + __uint_as_float(s[1]);
}
__device__ __forceinline__ float x16_max(float x) {
  auto s = __builtin_amdgcn_permlane16_swap(__float_as_uint(x), __float_as_uint(x), false, false);
  return fmaxf(__uint_as_float(s[0]), __uint_as_float(s[1]));
}
__device__ __forceinline__ float x32_max(float x) {
  auto s = __builtin_amdgcn_permlane32_swap(__float_as_uint(x), __float_as_uint(x), false, false);
  return fmaxf(__uint_as_float(s[0]), __uint_as_float(s[1]));
}
__device__ __forceinline__ void layernorm8(float (&v)[8]) {
  const float pv = __uint_as_float(__builtin_amdgcn_readfirstlane(__float_as_uint(v[0])));
  float d[8];
#pragma unroll
  for (int i = 0; i < 8; ++i) d[i] = v[i] - pv;
  float s = ((d[0] + d[1]) + (d[2] + d[3])) + ((d[4] + d[5]) + (d[6] + d[7]));
  float q = (fmaf(d[0], d[0], d[1] * d[1]) + fmaf(d[2], d[2], d[3] * d[3])) + (fmaf(d[4], d[4], d[5] * d[5]) + fmaf(d[6], d[6], d[7] * d[7]));
  s += dpp<XOR1>(s); q += dpp<XOR1>(q);
  s += dpp<XOR2>(s); q += dpp<XOR2>(q);
  s += dpp<HMIR>(s); q += dpp<HMIR>(q);
  s += dpp<MIR>(s);  q += dpp<MIR>(q);
  s = x16_sum(s); q = x16_sum(q);
  s = x32_sum(s) * (1.0f / 512.0f); q = x32_sum(q) * (1.0f / 512.0f);
  const float var = fmaxf(fmaf(-s, s, q), 0.f);
  const float mb = rb(pv + s);
  const float den = rb(__builtin_amdgcn_sqrtf(rb(rb(var) + 1e-5f)));
  const float inv = __builtin_amdgcn_rcpf(den);
#pragma unroll
  for (int i = 0; i < 8; ++i) v[i] = rb(v[i] - mb) * inv;
}

template <int IMM, int NT>
__device__ __forceinline__ void dma16(unsigned m0v, unsigned voff, rsrc_t srd, unsigned soff) {
  if constexpr (NT)
    asm volatile("s_mov_b32 m0, %0\n\ts_nop 0\n\tbuffer_load_dwordx4 %1, %2, %3 offen offset:%4 nt lds"
                 :: "s"(m0v), "v"(voff), "s"(srd), "s"(soff), "i"(IMM) : "memory");
  else
    asm volatile("s_mov_b32 m0, %0\n\ts_nop 0\n\tbuffer_load_dwordx4 %1, %2, %3 offen offset:%4 lds"
                 :: "s"(m0v), "v"(voff), "s"(srd), "s"(soff), "i"(IMM) : "memory");
}
template <int N> __device__ __forceinline__ void vmwait() { asm volatile("s_waitcnt vmcnt(%0)" :: "i"(N) : "memory"); }
#define LGKM0() asm volatile("s_waitcnt lgkmcnt(0)" ::: "memory")
__device__ __forceinline__ void wg_barrier() { LGKM0(); __builtin_amdgcn_s_barrier(); asm volatile("" ::: "memory"); }
#define PIN(x) asm volatile("" : "+v"(x))

__device__ __forceinline__ void flag_set(unsigned ldsb, int grp, int slot) {
  const unsigned a = ldsb + L_CT + grp * 16 + slot * 4, one = 1u;
  asm volatile("s_waitcnt lgkmcnt(0)\n\ts_mov_b64 exec, 1\n\tds_write_b32 %0, %1\n\ts_mov_b64 exec, -1" :: "v"(a), "v"(one) : "memory");
}
template <int N>
__device__ __forceinline__ void flag_wait(LAS unsigned char* L, int grp) {
  for (unsigned spins = 0; spins < (1u << 22); ++spins) {
    asm volatile("" ::: "memory");
    const v4u v = *(volatile LAS v4u*)(L + L_CT + grp * 16);
    const unsigned all = N == 4 ? (v.x & v.y & v.z & v.w) : (v.x & v.y);
    if (__builtin_amdgcn_readfirstlane(all)) break;
  }
  asm volatile("" ::: "memory");
}
__device__ __forceinline__ v4u ld128(LAS unsigned char* L, unsigned off) { return *(LAS v4u*)(L + off); }
__device__ __forceinline__ v4u tr_frag(LAS unsigned char* L, unsigned off, unsigned second) {
  const s16x4 a = __builtin_amdgcn_ds_read_tr16_b64_v4i16((LAS s16x4*)(L + off));
  const s16x4 b = __builtin_amdgcn_ds_read_tr16_b64_v4i16((LAS s16x4*)(L + off + second));
  const v2u x = __builtin_bit_cast(v2u, a), y = __builtin_bit_cast(v2u, b);
  return v4u{x.x, x.y, y.x, y.y};
}
__device__ __forceinline__ v4u gld128(rsrc_t rs, unsigned off) { return __builtin_amdgcn_raw_buffer_load_b128(rs, off, 0, 16); }
__device__ __forceinline__ void gst128(rsrc_t rs, unsigned off, v4u v) { __builtin_amdgcn_raw_buffer_store_b128(v, rs, off, 0, 16); }
__device__ __forceinline__ void gst64(rsrc_t rs, unsigned off, v2u v) { __builtin_amdgcn_raw_buffer_store_b64(v, rs, off, 0, 16); }

template <int N, int SLP>
__device__ __forceinline__ unsigned gather(rsrc_t ws, unsigned off, unsigned stride, unsigned tag, v4u (&v)[N]) {
  unsigned spins = 0;
  for (;; ++spins) {
    asm volatile("" ::: "memory");
#pragma unroll
    for (int k = 0; k < N; ++k) v[k] = gld128(ws, off + k * stride);
    bool ok = true;
#pragma unroll
    for (int k = 0; k < N; ++k) ok = ok && v[k].y == tag && v[k].w == tag;
    if (__all(ok) || spins >= SPIN_CAP) break;
    if (SLP) __builtin_amdgcn_s_sleep(SLP);
  }
  return spins + 1;
}

template <int N, int SLP>
__device__ __forceinline__ unsigned gather_m(rsrc_t ws, unsigned off, unsigned stride, unsigned tag, v4u (&v)[N]) {
#pragma unroll
  for (int k = 0; k < N; ++k) v[k] = gld128(ws, off + k * stride);
  unsigned spins = 1;
  for (;; ++spins) {
    bool need[N], any = false;
#pragma unroll
    for (int k = 0; k < N; ++k) { need[k] = !__all(v[k].y == tag && v[k].w == tag); any = any || need[k]; }
    if (!any || spins >= SPIN_CAP) break;
    if (SLP) __builtin_amdgcn_s_sleep(SLP);
    asm volatile("" ::: "memory");
#pragma unroll
    for (int k = 0; k < N; ++k) if (need[k]) v[k] = gld128(ws, off + k * stride);
  }
  return spins;
}

#define STAMP(i) do {} while (0)
#define COUNT(i, v) do { (void)(v); } while (0)

__global__ void __launch_bounds__(512, 2) fused_decode(Params p) {
  __shared__ __attribute__((aligned(16))) unsigned char lds_raw[LDS_BYTES];
  LAS unsigned char* L = (LAS unsigned char*)lds_raw;
  const unsigned ldsb = (unsigned)(size_t)L;
  STAMP(0);
  u64 code_base; asm volatile("s_getpc_b64 %0" : "=s"(code_base));
  asm volatile("" :: "s"(p.x), "s"(p.kc), "s"(p.vc), "s"(p.wq), "s"(p.wo), "s"(p.wg), "s"(p.wu), "s"(p.wd), "s"(p.out), "s"(p.ws));
  const int tid = threadIdx.x, lane = tid & 63;
  const int w = __builtin_amdgcn_readfirstlane(tid >> 6);
  const int c = blockIdx.x, hkv = c & 7, j = c >> 3, b = j >> 2, s = j & 3;
  const int g = lane >> 4, i16 = lane & 15;
  const rsrc_t ws = mk_rsrc(p.ws, WS_BYTES);
  unsigned tag = 0;
  const bool roleA = j < 16;
  const int hi4 = hkv >> 2;
  const bool roleB = j >= 16 && j - 16 < 8 - 2 * hi4;
  const int rot = (j + 3 * hkv) & 7;

  if (w >= 6) {
    const int l = w - 6;
    if (l == 1) {
      const rsrc_t rc = mk_rsrc((const void*)(code_base & ~(u64)255), CODE_BYTES);
      const unsigned vo = lane < 8 ? (unsigned)(j * 8 + lane) * 128u : 0x7fffffffu;
      asm volatile("s_nop 4\n\ts_mov_b32 m0, %0\n\ts_nop 0\n\tbuffer_load_dword %1, %2, 0 offen lds" :: "s"(ldsb + L_JUNK), "v"(vo), "s"(rc) : "memory");
    }
    if (!(roleA || roleB)) { vmwait<0>(); return; }
    __builtin_amdgcn_s_barrier();
    asm volatile("" ::: "memory");
    if (roleA) {
      const int head = 4 * hkv + (j >> 2), cb128 = j & 3;
      const rsrc_t ro = mk_rsrc((const char*)p.wo + (size_t)head * 128 * 1024 + cb128 * 256, 128 * 1024 - cb128 * 256);
      unsigned vo[2];
#pragma unroll
      for (int r = 0; r < 2; ++r) vo[r] = g * 1024 + ((i16 ^ (((4 * r + g) & 7) << 1)) * 16);
      flag_wait<2>(L, F_VI);
      flag_wait<4>(L, F_MAX);
      STAMP(1);
#pragma unroll 1
      for (int q = 0; q < 16; q += 2) {
        dma16<0, NT_W>(ldsb + L_K + (64 * l + 4 * q) * 256, vo[0], ro, (64 * l + 4 * q) * 1024);
        dma16<0, NT_W>(ldsb + L_K + (64 * l + 4 * q + 4) * 256, vo[1], ro, (64 * l + 4 * q + 4) * 1024);
      }
      STAMP(2);
    } else {
      const int m = (8 - 2 * hi4) * hkv + 8 * hi4 + (j - 16);
      const rsrc_t rg = mk_rsrc((const char*)(l == 0 ? p.wg : p.wu) + m * 64, 512 * FF * 2 - m * 64);
      const unsigned vg = (lane >> 2) * (FF * 2) + (((lane & 3) ^ (2 * ((lane >> 4) & 1) + l)) * 16);
      const rsrc_t rd = mk_rsrc((const char*)p.wd + (size_t)m * 32 * 1024, 32 * 1024);
      if (RW_START == 10) flag_wait<4>(L, F_PV); else flag_wait<2>(L, F_VI);
      flag_wait<4>(L, F_MAX);
      STAMP(1);
#pragma unroll 1
      for (int q = 0; q < 32; ++q) {
        const unsigned qq = (q + 5 * m) & 31;
        dma16<0, NT_W>(ldsb + L_G + l * 32768 + qq * 1024, vg, rg, qq * 16 * (FF * 2));
      }
      STAMP(2);
      flag_wait<4>(L, F_PV);
#pragma unroll 1
      for (int q = 0; q < 16; ++q) dma16<0, NT_W>(ldsb + L_V + (16 * l + q) * 1024, (unsigned)(lane ^ ((q & 7) << 1)) * 16, rd, (16 * l + q) * 1024);
      STAMP(3);
    }
    vmwait<0>();
    STAMP(4);
  } else if (w < 4) {
    const int qcb = j & 7, kq = j >> 3;
    {
      const rsrc_t rx = mk_rsrc(p.x, NB * DM * 2);
      const int n0 = hkv * 512 + 64 * qcb;
      const rsrc_t rq = mk_rsrc((const char*)p.wq + (size_t)kq * 128 * 8192 + n0 * 2, 128 * 8192 - n0 * 2);
      const unsigned vq = (lane >> 3) * 8192 + (((lane & 7) ^ (((lane >> 4) & 3) << 1)) * 16);
      asm volatile("s_nop 4\n\tbuffer_load_dword %0, off, %1, 0 sc1" : "=&v"(tag) : "s"(ws) : "memory");
      dma16<0, 0>(ldsb + L_XN + (2 * w) * XN_S, lane * 16, rx, (2 * w) * 1024);
      dma16<0, 0>(ldsb + L_XN + (2 * w + 1) * XN_S, lane * 16, rx, (2 * w + 1) * 1024);
#pragma unroll
      for (int q = 0; q < 4; ++q) dma16<0, NT_W>(ldsb + L_WQ + (32 * w + 8 * q) * 128, vq, rq, (32 * w + 8 * q) * 8192);
    }
    if (w == 0 && lane < 4 * F_N) ((LAS unsigned*)(L + L_CT))[lane] = 0u;
    wg_barrier();
    asm volatile("s_waitcnt vmcnt(4)" : "+v"(tag) :: "memory");
    STAMP(1);
    tag += 1u;
    if (w == 0 && lane == 0) ((LAS unsigned*)(L + L_CT))[4 * F_TAG] = tag;
    {
      const v4u raw0 = ld128(L, L_XN + (2 * w) * XN_S + lane * 16), raw1 = ld128(L, L_XN + (2 * w + 1) * XN_S + lane * 16);
      float v0[8] = {lo16(raw0.x), hi16(raw0.x), lo16(raw0.y), hi16(raw0.y), lo16(raw0.z), hi16(raw0.z), lo16(raw0.w), hi16(raw0.w)};
      float v1[8] = {lo16(raw1.x), hi16(raw1.x), lo16(raw1.y), hi16(raw1.y), lo16(raw1.z), hi16(raw1.z), lo16(raw1.w), hi16(raw1.w)};
      layernorm8(v0); layernorm8(v1);
      const int t = lane >> 2, q = lane & 3;
      const unsigned base = L_XN + (2 * w) * XN_S + (32 * t + 16 * (q & 1) + 4 * (q >> 1)) * 2;
      asm volatile("" ::: "memory");
      *(LAS v2u*)(L + base) = v2u{pk(v0[0], v0[1]), pk(v0[2], v0[3])};
      *(LAS v2u*)(L + base + 16) = v2u{pk(v0[4], v0[5]), pk(v0[6], v0[7])};
      *(LAS v2u*)(L + base + XN_S) = v2u{pk(v1[0], v1[1]), pk(v1[2], v1[3])};
      *(LAS v2u*)(L + base + XN_S + 16) = v2u{pk(v1[4], v1[5]), pk(v1[6], v1[7])};
    }
    vmwait<0>();
    STAMP(2);
    flag_set(ldsb, F_XW, w);
    const int rr = 4 * g + (i16 >> 2), pp = i16 & 3;
    {
      unsigned ab = L_WQ + rr * 128 + (((2 * w + (pp >> 1)) ^ (((rr >> 1) & 3) << 1)) * 16) + 8 * (pp & 1);
      unsigned bb = L_XN + (i16 & 7) * XN_S + (128 * kq + 8 * g) * 2;
      unsigned qa = WS_QG + (unsigned)(((hkv * 8 + 4 * (g & 1) + (g >> 1)) * 4 + kq) * 512 + 64 * qcb + 16 * w + i16) * 8;
      PIN(ab); PIN(bb); PIN(qa);
      flag_wait<4>(L, F_XW);
      v4u a[4], bq[4];
#pragma unroll
      for (int t = 0; t < 4; ++t) { a[t] = tr_frag(L, ab + t * 4096, 2048); bq[t] = ld128(L, bb + t * 64); }
      __builtin_amdgcn_sched_barrier(0);
      f32x4 acc = {0.f, 0.f, 0.f, 0.f};
#pragma unroll
      for (int t = 0; t < 4; ++t) acc = mfma(bq[t], a[t], acc);
      gst64(ws, qa, v2u{__float_as_uint(g < 2 ? acc[0] : acc[1]), tag});
      gst64(ws, qa + 2 * 4 * 512 * 8, v2u{__float_as_uint(g < 2 ? acc[2] : acc[3]), tag});
    }
    STAMP(3);
    {
      const unsigned off = WS_QG + (unsigned)((hkv * 8 + b) * 4 * 512 + 128 * w + 2 * lane) * 8;
      v4u v[4];
      flag_wait<2>(L, F_KI);
      { const unsigned n = gather_m<4, 2>(ws, off, 4096, tag, v); COUNT(16, n); }
      const float q0 = (__uint_as_float(v[0].x) + __uint_as_float(v[1].x)) + (__uint_as_float(v[2].x) + __uint_as_float(v[3].x));
      const float q1 = (__uint_as_float(v[0].z) + __uint_as_float(v[1].z)) + (__uint_as_float(v[2].z) + __uint_as_float(v[3].z));
      *(LAS unsigned*)(L + L_Q + w * Q_S + lane * 4) = pk(q0, q1);
    }
    flag_set(ldsb, F_Q, w);
    STAMP(4);
    float sc[4];
    const int hd = i16 & 3, dup = i16 >> 2;
    {
      flag_wait<2>(L, F_K);
      v4u kf[16];
#pragma unroll
      for (int T = 0; T < 4; ++T)
#pragma unroll
        for (int t = 0; t < 4; ++t) kf[4 * T + t] = ld128(L, L_K + (64 * w + 16 * T + i16) * 256 + (((4 * t + g) ^ i16) * 16));
      flag_wait<4>(L, F_Q);
      STAMP(5);
      v4u qf[4];
#pragma unroll
      for (int t = 0; t < 4; ++t) qf[t] = ld128(L, L_Q + hd * Q_S + (4 * t + g) * 16);
      __builtin_amdgcn_sched_barrier(0);
      f32x4 acc[4];
#pragma unroll
      for (int T = 0; T < 4; ++T) acc[T] = f32x4{0.f, 0.f, 0.f, 0.f};
#pragma unroll
      for (int t = 0; t < 4; ++t)
#pragma unroll
        for (int T = 0; T < 4; ++T) acc[T] = mfma(kf[4 * T + t], qf[t], acc[T]);
#pragma unroll
      for (int r = 0; r < 4; ++r) {
        const float a0 = acc[0][r], a1 = acc[1][r], a2 = acc[2][r], a3 = acc[3][r];
        const float a01 = (dup & 1) ? a1 : a0, a23 = (dup & 1) ? a3 : a2;
        sc[r] = rb(rb((dup & 2) ? a23 : a01) * SCALE);
      }
    }
    float inv, lse;
    {
      float v = fmaxf(fmaxf(sc[0], sc[1]), fmaxf(sc[2], sc[3]));
      v = fmaxf(v, dpp<ROR4>(v)); v = fmaxf(v, dpp<ROR8>(v));
      v = x32_max(x16_max(v));
      if (lane < 4) ((LAS float*)(L + L_ST))[lane * 4 + w] = v;
      flag_set(ldsb, F_MAX, w);
      flag_wait<4>(L, F_MAX);
      const f32x4 m4 = *(LAS f32x4*)(L + L_ST + hd * 16);
      const float mx = fmaxf(fmaxf(m4[0], m4[1]), fmaxf(m4[2], m4[3]));
      float e[4];
#pragma unroll
      for (int r = 0; r < 4; ++r) e[r] = __builtin_amdgcn_exp2f((sc[r] - mx) * LOG2E);
      *(LAS v2u*)(L + L_P + hd * P_S + (32 * (2 * w + (dup >> 1)) + 8 * g + 4 * (dup & 1)) * 2) = v2u{pk(e[0], e[1]), pk(e[2], e[3])};
      float t = (e[0] + e[1]) + (e[2] + e[3]);
      t += dpp<ROR4>(t); t += dpp<ROR8>(t);
      t = x32_sum(x16_sum(t));
      if (lane < 4) ((LAS float*)(L + L_ST))[16 + lane * 4 + w] = t;
      flag_set(ldsb, F_P, w);
      flag_wait<4>(L, F_P);
      const f32x4 l4 = *(LAS f32x4*)(L + L_ST + 64 + hd * 16), l4g = *(LAS f32x4*)(L + L_ST + 64 + g * 16);
      inv = __builtin_amdgcn_rcpf((l4g[0] + l4g[1]) + (l4g[2] + l4g[3]));
      lse = fmaf(mx, LOG2E, __builtin_amdgcn_logf((l4[0] + l4[1]) + (l4[2] + l4[3])));
    }
    STAMP(6);
    {
      const unsigned coblk = WS_CO + (unsigned)(hkv * 32 + j) * CO_BLK;
      if (w == 0 && lane < 16) gst64(ws, coblk + 2048 + lane * 8, v2u{__float_as_uint(lse), tag});
      v4u pf[8];
#pragma unroll
      for (int u = 0; u < 8; ++u) pf[u] = ld128(L, L_P + dup * P_S + (32 * ((u + rot) & 7) + 8 * g) * 2);
      const unsigned vb0 = L_V + rr * 256 + (((4 * w + (pp >> 1)) ^ ((rr & 7) << 1)) * 16) + 8 * (pp & 1);
      const unsigned vb1 = L_V + rr * 256 + (((4 * w + 2 + (pp >> 1)) ^ ((rr & 7) << 1)) * 16) + 8 * (pp & 1);
      unsigned oaddr = coblk + g * 512 + w * 128 + i16 * 8;
      PIN(oaddr); PIN(inv);
      f32x4 o0 = {0.f, 0.f, 0.f, 0.f}, o1 = {0.f, 0.f, 0.f, 0.f};
#pragma unroll
      for (int cc = 0; cc < 4; ++cc) {
        const unsigned ua = ((2 * cc + rot) & 7) * 8192, ub = ((2 * cc + 1 + rot) & 7) * 8192;
        unsigned a0 = vb0 + ua, a1 = vb1 + ua, b0 = vb0 + ub, b1 = vb1 + ub;
        PIN(a0); PIN(a1); PIN(b0); PIN(b1);
        flag_wait<2>(L, F_V + cc);
        const v4u a00 = tr_frag(L, a0, 4096), a10 = tr_frag(L, a1, 4096);
        const v4u a01 = tr_frag(L, b0, 4096), a11 = tr_frag(L, b1, 4096);
        __builtin_amdgcn_sched_barrier(0);
        o0 = mfma(pf[2 * cc], a00, o0); o1 = mfma(pf[2 * cc], a10, o1);
        o0 = mfma(pf[2 * cc + 1], a01, o0); o1 = mfma(pf[2 * cc + 1], a11, o1);
      }
      STAMP(7);
      const float va = o0[0] * inv, vb = o1[0] * inv;
      const float na = dpp<XOR1>(va), nb = dpp<XOR1>(vb);
      gst64(ws, oaddr, v2u{(lane & 1) ? pk(nb, vb) : pk(va, na), tag});
      if (roleB) flag_set(ldsb, F_PV, w);
    }
    STAMP(8);
  } else {
    const int l = w - 4;
    const size_t win = ((size_t)(b * NHKV + hkv) * SK + (SK - WIN) + 256 * s) * (DH * 2);
    const rsrc_t rk = mk_rsrc((const char*)p.kc + win, 65536), rv = mk_rsrc((const char*)p.vc + win, 65536);
    unsigned vk[4], vv[2];
#pragma unroll
    for (int r = 0; r < 4; ++r) vk[r] = g * 256 + ((i16 ^ (4 * r + g)) * 16);
    asm volatile("s_nop 4" ::: "memory");
    {
      const unsigned so = rot * 8192 + l * 4096;
      dma16<0, NT_KV>(ldsb + L_K + so, vk[0], rk, so); dma16<1024, NT_KV>(ldsb + L_K + so, vk[1], rk, so);
      dma16<2048, NT_KV>(ldsb + L_K + so, vk[2], rk, so); dma16<3072, NT_KV>(ldsb + L_K + so, vk[3], rk, so);
    }
#pragma unroll
    for (int r = 0; r < 2; ++r) vv[r] = g * 256 + ((i16 ^ (((4 * r + g) & 7) << 1)) * 16);
    __builtin_amdgcn_s_barrier();
    asm volatile("" ::: "memory");
    if (K_HOLD) vmwait<3>();
#pragma unroll 1
    for (int a = 1; a < 8; ++a) {
      const unsigned so = ((a + rot) & 7) * 8192 + l * 4096;
      vmwait<KV_WIN - 1>(); dma16<0, NT_KV>(ldsb + L_K + so, vk[0], rk, so);
      vmwait<KV_WIN - 1>(); dma16<1024, NT_KV>(ldsb + L_K + so, vk[1], rk, so);
      vmwait<KV_WIN - 1>(); dma16<2048, NT_KV>(ldsb + L_K + so, vk[2], rk, so);
      vmwait<KV_WIN - 1>(); dma16<3072, NT_KV>(ldsb + L_K + so, vk[3], rk, so);
    }
    if (Q_TRIG == 0) flag_set(ldsb, F_KI, l);
    STAMP(1);
#pragma unroll 1
    for (int a = 0; a < 8; ++a) {
      const unsigned so = ((a + rot) & 7) * 8192 + l * 4096;
      vmwait<KV_WIN - 1>(); dma16<0, NT_KV>(ldsb + L_V + so, vv[0], rv, so);
      vmwait<KV_WIN - 1>(); dma16<1024, NT_KV>(ldsb + L_V + so, vv[1], rv, so);
      vmwait<KV_WIN - 1>(); dma16<2048, NT_KV>(ldsb + L_V + so, vv[0], rv, so);
      vmwait<KV_WIN - 1>(); dma16<3072, NT_KV>(ldsb + L_V + so, vv[1], rv, so);
      if (a == 2) { vmwait<(KV_WIN < 12 ? KV_WIN : 12)>(); flag_set(ldsb, F_K, l); STAMP(2); }
      if (Q_TRIG > 0 && a == Q_TRIG - 1) flag_set(ldsb, F_KI, l);
      if (a == (roleA ? RWA_START : RW_START) - 1) flag_set(ldsb, F_VI, l);
    }
    STAMP(3);
    if (KV_WIN > 24) vmwait<24>();
    flag_set(ldsb, F_V + 0, l);
    if (KV_WIN > 16) vmwait<16>();
    flag_set(ldsb, F_V + 1, l);
    vmwait<8>(); flag_set(ldsb, F_V + 2, l);
    vmwait<0>(); flag_set(ldsb, F_V + 3, l);
    if ((roleA ? RWA_START : RW_START) == 9) flag_set(ldsb, F_VI, l);
    STAMP(4);
  }

  const int rr = 4 * g + (i16 >> 2), pp = i16 & 3;
  if (roleA) {
    const int hl = j >> 2, cb128 = j & 3, head = 4 * hkv + hl;
    const unsigned pb = WS_CO + (unsigned)(hkv * 32 + 4 * w) * CO_BLK;
    const unsigned o0 = pb + (lane >> 5) * CO_BLK + hl * 512 + (lane & 31) * 16;
    const unsigned os = pb + (lane & 3) * CO_BLK + 2048 + hl * 8;
    const int ch = lane & 31;
    unsigned owr = L_O + w * O_S + (32 * (ch >> 3) + 8 * ((ch & 7) >> 1) + 2 * (ch & 1)) * 2;
    unsigned rpaddr = WS_RP + (unsigned)((head * 8 + 4 * (g & 1) + (g >> 1)) * 512 + cb128 * 128 + 16 * w + i16) * 8;
    unsigned obase = L_O + (i16 & 7) * O_S + 8 * g * 2;
    unsigned ab = L_K + rr * 256 + (((2 * w + (pp >> 1)) ^ ((rr & 7) << 1)) * 16) + 8 * (pp & 1);
    PIN(owr); PIN(rpaddr); PIN(obase); PIN(ab);
    wg_barrier();
    tag = ((LAS unsigned*)(L + L_CT))[4 * F_TAG];
    STAMP(9);
    v4u af[4];
    {
      if (CO_DLY) __builtin_amdgcn_s_sleep(CO_DLY);
      v4u v0 = gld128(ws, o0), v1 = gld128(ws, o0 + 2 * CO_BLK);
      v2u st = __builtin_amdgcn_raw_buffer_load_b64(ws, os, 0, 16);
#pragma unroll
      for (int t = 0; t < 4; ++t) af[t] = tr_frag(L, ab + t * 8192, 4096);
      unsigned npass = 1;
      for (;;) {
        const bool n0 = !__all(v0.y == tag && v0.w == tag), n1 = !__all(v1.y == tag && v1.w == tag), n2 = !__all(st.y == tag);
        if (!(n0 || n1 || n2) || npass >= SPIN_CAP) break;
        if (CO_SLP) __builtin_amdgcn_s_sleep(CO_SLP);
        asm volatile("" ::: "memory");
        if (n0 || !MASKED) v0 = gld128(ws, o0);
        if (n1 || !MASKED) v1 = gld128(ws, o0 + 2 * CO_BLK);
        if (n2 || !MASKED) st = __builtin_amdgcn_raw_buffer_load_b64(ws, os, 0, 16);
        ++npass;
      }
      COUNT(17, npass);
      STAMP(10);
      const float Ls = __uint_as_float(st.x);
      float Lm = fmaxf(Ls, dpp<XOR1>(Ls)); Lm = fmaxf(Lm, dpp<XOR2>(Lm));
      const float e = __builtin_amdgcn_exp2f(Ls - Lm);
      float es = e + dpp<XOR1>(e); es += dpp<XOR2>(es);
      const float wt = e * __builtin_amdgcn_rcpf(es);
      const float w0 = dpp<0x00>(wt), w1 = dpp<0x55>(wt), w2 = dpp<0xAA>(wt), w3 = dpp<0xFF>(wt);
      const float wa = lane < 32 ? w0 : w1, wb = lane < 32 ? w2 : w3;
      const float a0 = x32_sum(fmaf(wa, lo16(v0.x), wb * lo16(v1.x))), a1 = x32_sum(fmaf(wa, hi16(v0.x), wb * hi16(v1.x)));
      const float a2 = x32_sum(fmaf(wa, lo16(v0.z), wb * lo16(v1.z))), a3 = x32_sum(fmaf(wa, hi16(v0.z), wb * hi16(v1.z)));
      if (lane < 32) { *(LAS unsigned*)(L + owr) = pk(a0, a1); *(LAS unsigned*)(L + owr + 8) = pk(a2, a3); }
    }
    wg_barrier();
    {
      v4u bo[4];
#pragma unroll
      for (int t = 0; t < 4; ++t) bo[t] = ld128(L, obase + t * 64);
      __builtin_amdgcn_sched_barrier(0);
      f32x4 acc = {0.f, 0.f, 0.f, 0.f};
#pragma unroll
      for (int t = 0; t < 4; ++t) acc = mfma(bo[t], af[t], acc);
      gst64(ws, rpaddr, v2u{__float_as_uint(g < 2 ? acc[0] : acc[1]), tag});
      gst64(ws, rpaddr + 2 * 512 * 8, v2u{__float_as_uint(g < 2 ? acc[2] : acc[3]), tag});
    }
    STAMP(11);
    if (w == 0) {
      const int br = j & 7, cb32 = (j >> 3) * 8 + hkv;
      const unsigned xr = ((const unsigned*)p.x)[br * 256 + cb32 * 16 + i16];
      const unsigned off = WS_RP + (unsigned)((g * 8 + br) * 512 + 32 * cb32 + 2 * i16) * 8;
      const unsigned hoff = WS_HG + (unsigned)(br * 256 + 16 * cb32 + i16) * 8;
      if (R_DLY) __builtin_amdgcn_s_sleep(R_DLY);
      v4u v[8];
      { const unsigned n = MASKED ? gather_m<8, R_SLP>(ws, off, 4u * 8 * 512 * 8, tag, v) : gather<8, R_SLP>(ws, off, 4u * 8 * 512 * 8, tag, v); COUNT(18, n); }
      STAMP(12);
      float s0 = ((__uint_as_float(v[0].x) + __uint_as_float(v[1].x)) + (__uint_as_float(v[2].x) + __uint_as_float(v[3].x))) +
                 ((__uint_as_float(v[4].x) + __uint_as_float(v[5].x)) + (__uint_as_float(v[6].x) + __uint_as_float(v[7].x)));
      float s1 = ((__uint_as_float(v[0].z) + __uint_as_float(v[1].z)) + (__uint_as_float(v[2].z) + __uint_as_float(v[3].z))) +
                 ((__uint_as_float(v[4].z) + __uint_as_float(v[5].z)) + (__uint_as_float(v[6].z) + __uint_as_float(v[7].z)));
      s0 = x32_sum(x16_sum(s0)); s1 = x32_sum(x16_sum(s1));
      const unsigned hp = pk(lo16(xr) + rb(s0), hi16(xr) + rb(s1));
      if (lane < 16) gst64(ws, hoff, v2u{hp, tag});
      STAMP(13);
    } else if (w == 1) {
      const int a = hkv * 16 + j, grp = lane >> 3, bb8 = lane & 7;
      const unsigned off = WS_R2 + (unsigned)grp * 16384 + (a >> 3) * 1024 + (a & 3) * 256 + ((a >> 2) & 1) * 128 + bb8 * 16;
      __builtin_amdgcn_s_sleep(32);
      v4u hv[1];
      gather<1, 8>(ws, WS_HG + (unsigned)(bb8 * 256 + 2 * a) * 8, 0, tag, hv);
      v4u v[7];
      { const unsigned n = MASKED ? gather_m<7, R2_SLP>(ws, off, 8u * 16384, tag, v) : gather<7, R2_SLP>(ws, off, 8u * 16384, tag, v); COUNT(19, n); }
      STAMP(14);
      float s0 = lo16(v[0].x), s1 = hi16(v[0].x), s2 = lo16(v[0].z), s3 = hi16(v[0].z);
#pragma unroll
      for (int k = 1; k < 7; ++k) { s0 += lo16(v[k].x); s1 += hi16(v[k].x); s2 += lo16(v[k].z); s3 += hi16(v[k].z); }
      s0 += dpp<ROR8>(s0); s1 += dpp<ROR8>(s1); s2 += dpp<ROR8>(s2); s3 += dpp<ROR8>(s3);
      s0 = x32_sum(x16_sum(s0)); s1 = x32_sum(x16_sum(s1)); s2 = x32_sum(x16_sum(s2)); s3 = x32_sum(x16_sum(s3));
      if (lane < 8)
        ((v2u*)p.out)[bb8 * 128 + a] = v2u{pk(lo16(hv[0].x) + rb(s0), hi16(hv[0].x) + rb(s1)), pk(lo16(hv[0].z) + rb(s2), hi16(hv[0].z) + rb(s3))};
      if (c == 0 && lane == 0) __builtin_amdgcn_raw_buffer_store_b32(tag, ws, WS_E, 0, 16);
      STAMP(15);
    }
  } else if (roleB) {
    const int m = (8 - 2 * hi4) * hkv + 8 * hi4 + (j - 16);
    unsigned r2addr = WS_R2 + (unsigned)m * 16384 + 2 * w * 1024 + lane * 16;
    unsigned bb = L_XN + (i16 & 7) * XN_S + 8 * g * 2;
    unsigned hnw = L_XN + w * XN_S + (32 * (lane >> 3) + 8 * (lane & 3) + 4 * ((lane >> 2) & 1)) * 2;
    unsigned hhr = L_HH + (i16 & 7) * HH_S + g * 16;
    unsigned hhw = L_HH + i16 * HH_S + (8 * ((2 * w + g) & 3) + 4 * ((2 * w + g) >> 2)) * 2;
    const unsigned hoff = WS_HG + (unsigned)(w * 256 + 2 * lane) * 8;
    PIN(r2addr); PIN(bb); PIN(hnw); PIN(hhr); PIN(hhw);
    wg_barrier();
    tag = ((LAS unsigned*)(L + L_CT))[4 * F_TAG];
    STAMP(9);
    v4u df[4], gf[16];
#pragma unroll
    for (int x = 0; x < 4; ++x)
      df[x] = tr_frag(L, L_V + rr * 1024 + (((2 * (4 * w + x) + (pp >> 1)) ^ ((rr & 7) << 1)) * 16) + 8 * (pp & 1), 16 * 1024);
    if (w < 4) {
      const int mat = pp >> 1;
      const unsigned gb = L_G + mat * 32768 + rr * 64 + ((w ^ (2 * ((rr >> 2) & 1) + mat)) * 16) + 8 * (pp & 1);
#pragma unroll
      for (int t = 0; t < 16; ++t) gf[t] = tr_frag(L, gb + t * 2048, 1024);
    }
    {
      v4u hv[2];
      { const unsigned n = MASKED ? gather_m<2, H_SLP>(ws, hoff, 1024, tag, hv) : gather<2, H_SLP>(ws, hoff, 1024, tag, hv); COUNT(20, n); }
      STAMP(10);
      float v[8] = {lo16(hv[0].x), hi16(hv[0].x), lo16(hv[0].z), hi16(hv[0].z), lo16(hv[1].x), hi16(hv[1].x), lo16(hv[1].z), hi16(hv[1].z)};
      layernorm8(v);
      *(LAS v2u*)(L + hnw) = v2u{pk(v[0], v[1]), pk(v[2], v[3])};
      *(LAS v2u*)(L + hnw + 512) = v2u{pk(v[4], v[5]), pk(v[6], v[7])};
    }
    wg_barrier();
    STAMP(11);
    if (w < 4) {
      v4u hb[16];
#pragma unroll
      for (int t = 0; t < 16; ++t) hb[t] = ld128(L, bb + t * 64);
      __builtin_amdgcn_sched_barrier(0);
      f32x4 acc = {0.f, 0.f, 0.f, 0.f}, acc2 = {0.f, 0.f, 0.f, 0.f};
#pragma unroll
      for (int t = 0; t < 16; t += 2) { acc = mfma(gf[t], hb[t], acc); acc2 = mfma(gf[t + 1], hb[t + 1], acc2); }
      float hh[4];
#pragma unroll
      for (int r = 0; r < 4; ++r) {
        const float v = rb(acc[r] + acc2[r]);
        auto sw = __builtin_amdgcn_permlane32_swap(__float_as_uint(v), __float_as_uint(v), false, false);
        const float gt = __uint_as_float(sw[0]), up = __uint_as_float(sw[1]);
        const float sg = rb(__builtin_amdgcn_rcpf(1.0f + __builtin_amdgcn_exp2f(-gt * LOG2E)));
        hh[r] = sg * up;
      }
      if (lane < 32 && i16 < 8) *(LAS v2u*)(L + hhw) = v2u{pk(hh[0], hh[1]), pk(hh[2], hh[3])};
    }
    wg_barrier();
    STAMP(12);
    {
      const v4u bf = ld128(L, hhr);
      const int half = i16 >> 3;
      f32x4 acc[4];
#pragma unroll
      for (int x = 0; x < 4; ++x) acc[x] = mfma(df[x], bf, f32x4{0.f, 0.f, 0.f, 0.f});
#pragma unroll
      for (int y = 0; y < 2; ++y) {
        const f32x4 e = acc[2 * y], d = acc[2 * y + 1];
        const float o0 = half ? d[0] : e[0], o1 = half ? d[1] : e[1], o2 = half ? d[2] : e[2], o3 = half ? d[3] : e[3];
        gst128(ws, r2addr + y * 1024, v4u{pk(o0, o1), tag, pk(o2, o3), tag});
      }
    }
    STAMP(13);
  }
}
}

extern "C" void kernel_launch(void* const* d_in, const int* in_sizes, int n_in, void* d_out, int out_size,
                              void* d_ws, size_t ws_size, hipStream_t stream) {
  fk::Params p;
  p.x = d_in[0]; p.kc = d_in[1]; p.vc = d_in[2]; p.wq = d_in[3]; p.wo = d_in[4];
  p.wg = d_in[6]; p.wu = d_in[7]; p.wd = d_in[8]; p.out = d_out; p.ws = d_ws;
  hipLaunchKernelGGL(fk::fused_decode, dim3(256), dim3(512), 0, stream, p);
}
```

```cpp
#include <hip/hip_runtime.h>
#include <hip/hip_bf16.h>
#include <stdint.h>

#define CODE_BYTES 15360

namespace fk {
#define LAS __attribute__((address_space(3)))
typedef unsigned v4u __attribute__((ext_vector_type(4)));
typedef unsigned v2u __attribute__((ext_vector_type(2)));
typedef float f32x4 __attribute__((ext_vector_type(4)));
typedef float f32x2 __attribute__((ext_vector_type(2)));
typedef short s16x4 __attribute__((ext_vector_type(4)));
typedef __bf16 bf16x8 __attribute__((ext_vector_type(8)));
typedef __bf16 bf16x2 __attribute__((ext_vector_type(2)));
typedef unsigned long long u64;
typedef __amdgpu_buffer_rsrc_t rsrc_t;

constexpr int NB = 8, DM = 512, NHKV = 8, SK = 4096, WIN = 1024, DH = 128, FF = 1792;
constexpr int NMLP = 56;
constexpr float SCALE = 0.08838834764831845f;
constexpr float LOG2E = 1.4426950408889634f;
constexpr unsigned SPIN_CAP = 1u << 17;

constexpr int NT_KV = 1, NT_W = 1, NT_G = 0;
constexpr int KV_WIN = 11;
constexpr int K_FIRST = 2;
constexpr int QD = 44;
constexpr int RWA_START = 7;
constexpr int CO_DLY = 6, R_DLY = 16;
constexpr int R_SLP = 1, H_SLP = 3, R2_SLP = 1, RSLP = 3;

constexpr unsigned WS_E = 0;
constexpr unsigned WS_QG = 4096;
constexpr unsigned WS_CO = WS_QG + 8 * 8 * 4 * 512 * 4;
constexpr unsigned CO_BLK = 2176;
constexpr unsigned WS_RP = WS_CO + 256 * CO_BLK;
constexpr unsigned WS_HG = WS_RP + 32 * 8 * 512 * 4;
constexpr unsigned HG_N = 8, HG_COPY = 8 * 256 * 8;
constexpr unsigned WS_R2 = WS_HG + HG_N * HG_COPY;
constexpr unsigned WS_END = WS_R2 + NMLP * 16384;
constexpr unsigned WS_BYTES = WS_END;

constexpr unsigned L_K = 0, L_V = 65536, L_WQ = 131072, L_XN = 147456, XN_S = 1056;
constexpr unsigned L_Q = L_XN + 8 * XN_S, Q_S = 320;
constexpr unsigned L_P = L_Q + 4 * Q_S, P_S = 576;
constexpr unsigned L_JUNK = L_P + 4 * P_S;
constexpr unsigned L_ST = L_JUNK + 256;
constexpr unsigned L_CT = L_ST + 128;
constexpr unsigned LDS_BYTES = L_CT + 256;
static_assert(LDS_BYTES <= 163840, "LDS");
constexpr unsigned L_O = L_WQ, O_S = 288;
constexpr unsigned L_HH = L_WQ + 8192, HH_S = 96;
constexpr unsigned L_W = L_WQ + 4096;
constexpr unsigned L_G = L_K;
enum { F_XW = 0, F_Q = 1, F_P = 2, F_PV = 3, F_K = 4, F_VI = 5, F_V = 6  , F_RE = 10, F_TAG = 11 };

struct Params {
  const void* x; const void* kc; const void* vc; const void* wq; const void* wo;
  const void* wg; const void* wu; const void* wd; void* out; void* ws;
};

__device__ __forceinline__ rsrc_t mk_rsrc(const void* p, unsigned bytes) {
  return __builtin_amdgcn_make_buffer_rsrc((void*)p, 0, bytes, 0x00020000);
}
__device__ __forceinline__ unsigned pk(float a, float b) { bf16x2 v = {(__bf16)a, (__bf16)b}; return __builtin_bit_cast(unsigned, v); }
__device__ __forceinline__ float rb(float a) { return (float)(__bf16)a; }
__device__ __forceinline__ float lo16(unsigned u) { return __uint_as_float(u << 16); }
__device__ __forceinline__ float hi16(unsigned u) { return __uint_as_float(u & 0xffff0000u); }
__device__ __forceinline__ f32x4 mfma(v4u a, v4u b, f32x4 c) {
  return __builtin_amdgcn_mfma_f32_16x16x32_bf16(__builtin_bit_cast(bf16x8, a), __builtin_bit_cast(bf16x8, b), c, 0, 0, 0);
}
template <int CTRL> __device__ __forceinline__ float dpp(float x) {
  return __builtin_bit_cast(float, __builtin_amdgcn_mov_dpp(__builtin_bit_cast(int, x), CTRL, 0xf, 0xf, true));
}
constexpr int XOR1 = 0xB1, XOR2 = 0x4E, HMIR = 0x141, MIR = 0x140, ROR4 = 0x124, ROR8 = 0x128;
__device__ __forceinline__ float x16_sum(float x) {
  auto s = __builtin_amdgcn_permlane16_swap(__float_as_uint(x), __float_as_uint(x), false, false);
  return __uint_as_float(s[0]) + __uint_as_float(s[1]);
}
__device__ __forceinline__ float x32_sum(float x) {
  auto s = __builtin_amdgcn_permlane32_swap(__float_as_uint(x), __float_as_uint(x), false, false);
  return __uint_as_float(s[0]) + __uint_as_float(s[1]);
}
__device__ __forceinline__ float x16_max(float x) {
  auto s = __builtin_amdgcn_permlane16_swap(__float_as_uint(x), __float_as_uint(x), false, false);
  return fmaxf(__uint_as_float(s[0]), __uint_as_float(s[1]));
}
__device__ __forceinline__ float x32_max(float x) {
  auto s = __builtin_amdgcn_permlane32_swap(__float_as_uint(x), __float_as_uint(x), false, false);
  return fmaxf(__uint_as_float(s[0]), __uint_as_float(s[1]));
}
__device__ __forceinline__ v4u layernorm8(const v4u raw) {
  const f32x2 a0 = {lo16(raw.x), hi16(raw.x)}, a1 = {lo16(raw.y), hi16(raw.y)}, a2 = {lo16(raw.z), hi16(raw.z)}, a3 = {lo16(raw.w), hi16(raw.w)};
  const float pv = __uint_as_float(__builtin_amdgcn_readfirstlane(__float_as_uint(a0.x)));
  const f32x2 P = {pv, pv};
  const f32x2 d0 = a0 - P, d1 = a1 - P, d2 = a2 - P, d3 = a3 - P;
  const f32x2 s2 = (d0 + d1) + (d2 + d3);
  const f32x2 q2 = __builtin_elementwise_fma(d0, d0, d1 * d1) + __builtin_elementwise_fma(d2, d2, d3 * d3);
  float s = s2.x + s2.y, q = q2.x + q2.y;
  s += dpp<XOR1>(s); q += dpp<XOR1>(q);
  s += dpp<XOR2>(s); q += dpp<XOR2>(q);
  s += dpp<HMIR>(s); q += dpp<HMIR>(q);
  s += dpp<MIR>(s);  q += dpp<MIR>(q);
  s = x16_sum(s); q = x16_sum(q);
  s = x32_sum(s) * (1.0f / 512.0f); q = x32_sum(q) * (1.0f / 512.0f);
  const float var = fmaxf(fmaf(-s, s, q), 0.f);
  const float mb = rb(pv + s);
  const float inv = __builtin_amdgcn_rcpf(rb(__builtin_amdgcn_sqrtf(rb(rb(var) + 1e-5f))));
  const f32x2 M = {mb, mb}, I = {inv, inv};
  const f32x2 e0 = a0 - M, e1 = a1 - M, e2 = a2 - M, e3 = a3 - M;
  const unsigned r0 = pk(e0.x, e0.y), r1 = pk(e1.x, e1.y), r2 = pk(e2.x, e2.y), r3 = pk(e3.x, e3.y);
  const f32x2 f0 = f32x2{lo16(r0), hi16(r0)} * I, f1 = f32x2{lo16(r1), hi16(r1)} * I, f2 = f32x2{lo16(r2), hi16(r2)} * I, f3 = f32x2{lo16(r3), hi16(r3)} * I;
  return v4u{pk(f0.x, f0.y), pk(f1.x, f1.y), pk(f2.x, f2.y), pk(f3.x, f3.y)};
}

template <int IMM, int NT>
__device__ __forceinline__ void dma16(unsigned m0v, unsigned voff, rsrc_t srd, unsigned soff) {
  if constexpr (NT)
    asm volatile("s_mov_b32 m0, %0\n\ts_nop 0\n\tbuffer_load_dwordx4 %1, %2, %3 offen offset:%4 nt lds"
                 :: "s"(m0v), "v"(voff), "s"(srd), "s"(soff), "i"(IMM) : "memory");
  else
    asm volatile("s_mov_b32 m0, %0\n\ts_nop 0\n\tbuffer_load_dwordx4 %1, %2, %3 offen offset:%4 lds"
                 :: "s"(m0v), "v"(voff), "s"(srd), "s"(soff), "i"(IMM) : "memory");
}
template <int N> __device__ __forceinline__ void vmwait() { asm volatile("s_waitcnt vmcnt(%0)" :: "i"(N) : "memory"); }
template <int N> __device__ __forceinline__ unsigned tag_after(unsigned e_raw) {
  unsigned t;
  asm volatile("s_waitcnt vmcnt(%2)\n\tv_add_u32 %0, 1, %1" : "=v"(t) : "v"(e_raw), "i"(N) : "memory");
  return t;
}
#define LGKM0() asm volatile("s_waitcnt lgkmcnt(0)" ::: "memory")
__device__ __forceinline__ void wg_barrier() { LGKM0(); __builtin_amdgcn_s_barrier(); asm volatile("" ::: "memory"); }
#define PIN(x) asm volatile("" : "+v"(x))

__device__ __forceinline__ void flag_set(unsigned ldsb, int grp, int slot) {
  const unsigned a = ldsb + L_CT + grp * 16 + slot * 4, one = 1u;
  asm volatile("s_waitcnt lgkmcnt(0)\n\ts_mov_b64 exec, 1\n\tds_write_b32 %0, %1\n\ts_mov_b64 exec, -1" :: "v"(a), "v"(one) : "memory");
}
template <int N, int SLP = 0>
__device__ __forceinline__ void flag_wait(LAS unsigned char* L, int grp) {
#pragma unroll 1
  for (unsigned spins = 0; spins < (1u << 22); ++spins) {
    asm volatile("" ::: "memory");
    const v4u v = *(volatile LAS v4u*)(L + L_CT + grp * 16);
    const unsigned all = N == 4 ? (v.x & v.y & v.z & v.w) : (v.x & v.y);
    if (__builtin_amdgcn_readfirstlane(all)) break;
    if (SLP) __builtin_amdgcn_s_sleep(SLP);
  }
  asm volatile("" ::: "memory");
}
__device__ __forceinline__ unsigned init_mark(unsigned tag) { return tag * 0x9e3779b1u + 0x7f4a7c15u; }
__device__ __forceinline__ void init_wait(LAS unsigned char* L, unsigned tag) {
  const unsigned want = __builtin_amdgcn_readfirstlane(init_mark(tag));
#pragma unroll 1
  for (unsigned spins = 0; spins < (1u << 22); ++spins) {
    asm volatile("" ::: "memory");
    const unsigned v = *(volatile LAS unsigned*)(L + L_CT + F_TAG * 16);
    if ((unsigned)__builtin_amdgcn_readfirstlane(v) == want) break;
  }
  asm volatile("" ::: "memory");
}
__device__ __forceinline__ v4u ld128(LAS unsigned char* L, unsigned off) { return *(LAS v4u*)(L + off); }
__device__ __forceinline__ v4u tr_frag(LAS unsigned char* L, unsigned off, unsigned second) {
  const s16x4 a = __builtin_amdgcn_ds_read_tr16_b64_v4i16((LAS s16x4*)(L + off));
  const s16x4 b = __builtin_amdgcn_ds_read_tr16_b64_v4i16((LAS s16x4*)(L + off + second));
  const v2u x = __builtin_bit_cast(v2u, a), y = __builtin_bit_cast(v2u, b);
  return v4u{x.x, x.y, y.x, y.y};
}
__device__ __forceinline__ v4u gld128(rsrc_t rs, unsigned off) { return __builtin_amdgcn_raw_buffer_load_b128(rs, off, 0, 16); }
__device__ __forceinline__ void gst128(rsrc_t rs, unsigned off, v4u v) { __builtin_amdgcn_raw_buffer_store_b128(v, rs, off, 0, 16); }
__device__ __forceinline__ void gst64(rsrc_t rs, unsigned off, v2u v) { __builtin_amdgcn_raw_buffer_store_b64(v, rs, off, 0, 16); }

template <int N> __device__ __forceinline__ bool stale(const v4u (&v)[N], unsigned tag) {
  unsigned bad = (v[0].y ^ tag) | (v[0].w ^ tag);
#pragma unroll
  for (int k = 1; k < N; ++k) bad |= (v[k].y ^ tag) | (v[k].w ^ tag);
  return __builtin_amdgcn_ballot_w64(bad != 0u) != 0ull;
}
template <int N> __device__ __forceinline__ void gpass(rsrc_t ws, unsigned off, unsigned stride, v4u (&v)[N]) {
  asm volatile("" ::: "memory");
#pragma unroll
  for (int k = 0; k < N; ++k) v[k] = gld128(ws, off + k * stride);
}
template <int N, int SLP>
__device__ __forceinline__ unsigned gather(rsrc_t ws, unsigned off, unsigned stride, unsigned tag, v4u (&v)[N]) {
  unsigned spins = 1;
  gpass<N>(ws, off, stride, v);
  while (stale<N>(v, tag) && spins < SPIN_CAP) {
    if (SLP) __builtin_amdgcn_s_sleep(SLP);
    gpass<N>(ws, off, stride, v);
    ++spins;
  }
  return spins;
}
constexpr unsigned TM = 0xfu;
__device__ __forceinline__ unsigned tw(float v, unsigned tag) { return ((__float_as_uint(v) + (TM + 1) / 2) & ~TM) | (tag & TM); }
__device__ __forceinline__ float tw_val(unsigned u) { return __uint_as_float(u & ~TM); }
__device__ __forceinline__ void gst32(rsrc_t rs, unsigned off, unsigned v) { __builtin_amdgcn_raw_buffer_store_b32(v, rs, off, 0, 16); }
__device__ __forceinline__ v2u gld64(rsrc_t rs, unsigned off) { return __builtin_amdgcn_raw_buffer_load_b64(rs, off, 0, 16); }

__global__ void __launch_bounds__(512, 2) fused_decode(Params p) {
  __shared__ __attribute__((aligned(16))) unsigned char lds_raw[LDS_BYTES];
  LAS unsigned char* L = (LAS unsigned char*)lds_raw;
  const unsigned ldsb = (unsigned)(size_t)L;
  u64 code_base; asm volatile("s_getpc_b64 %0" : "=s"(code_base));
  asm volatile("" :: "s"(p.x), "s"(p.kc), "s"(p.vc), "s"(p.wq), "s"(p.wo), "s"(p.wg), "s"(p.wu), "s"(p.wd), "s"(p.out), "s"(p.ws));
  const int tid = threadIdx.x, lane = tid & 63;
  const int w = __builtin_amdgcn_readfirstlane(tid >> 6);
  const int c = blockIdx.x, hkv = c & 7, j = c >> 3, b = j >> 2, s = j & 3;
  const int g = lane >> 4, i16 = lane & 15;
  const rsrc_t ws = mk_rsrc(p.ws, WS_BYTES);
  unsigned tag = 0;
  const bool roleA = j < 16;
  const int hi4 = hkv >> 2;
  const bool roleB = j >= 16 && j - 16 < 8 - 2 * hi4;
  const int rot = (j + 3 * hkv) & 7;
  const int rotv = rot & 6;

  unsigned e_raw;
  asm volatile("s_nop 4\n\tbuffer_load_dword %0, off, %1, 0 sc1" : "=&v"(e_raw) : "s"(ws) : "memory");
  const size_t win = ((size_t)(b * NHKV + hkv) * SK + (SK - WIN) + 256 * s) * (DH * 2);
  if (w >= 6) {
    if (w == 7) {
      const rsrc_t rc = mk_rsrc((const void*)(code_base & ~(u64)255), CODE_BYTES);
      const unsigned vo = lane < 8 ? (unsigned)(j * 8 + lane) * 128u : 0x7fffffffu;
      asm volatile("s_nop 4\n\ts_mov_b32 m0, %0\n\ts_nop 0\n\tbuffer_load_dword %1, %2, 0 offen lds" :: "s"(ldsb + L_JUNK), "v"(vo), "s"(rc) : "memory");
    }
    if (!(roleA || roleB)) { vmwait<0>(); return; }
  } else if (w < 4) {
    const int qcb = j & 7, kq = j >> 3;
    const rsrc_t rx = mk_rsrc(p.x, NB * DM * 2);
    const int n0 = hkv * 512 + 64 * qcb;
    const rsrc_t rq = mk_rsrc((const char*)p.wq + (size_t)kq * 128 * 8192 + n0 * 2, 128 * 8192 - n0 * 2);
    const unsigned vq = (lane >> 3) * 8192 + (((lane & 7) ^ (((lane >> 4) & 3) << 1)) * 16);
    asm volatile("s_nop 4" ::: "memory");
    dma16<0, 0>(ldsb + L_XN + (2 * w) * XN_S, lane * 16, rx, (2 * w) * 1024);
    dma16<0, 0>(ldsb + L_XN + (2 * w + 1) * XN_S, lane * 16, rx, (2 * w + 1) * 1024);
#pragma unroll
    for (int q = 0; q < 4; ++q) dma16<0, NT_W>(ldsb + L_WQ + (32 * w + 8 * q) * 128, vq, rq, (32 * w + 8 * q) * 8192);
  } else {
    const rsrc_t rk = mk_rsrc((const char*)p.kc + win, 65536);
    unsigned vk[4];
#pragma unroll
    for (int r = 0; r < 4; ++r) vk[r] = g * 256 + ((i16 ^ (4 * r + g)) * 16);
    asm volatile("s_nop 4" ::: "memory");
#pragma unroll 1
    for (int a = 0; a < K_FIRST; ++a) {
      const unsigned so = ((a + rot) & 7) * 8192 + (w - 4) * 4096;
      dma16<0, NT_KV>(ldsb + L_K + so, vk[0], rk, so); dma16<1024, NT_KV>(ldsb + L_K + so, vk[1], rk, so);
      dma16<2048, NT_KV>(ldsb + L_K + so, vk[2], rk, so); dma16<3072, NT_KV>(ldsb + L_K + so, vk[3], rk, so);
    }
  }

  if (w >= 6) {
    const int l = w - 6;
    tag = tag_after<0>(e_raw);
    init_wait(L, tag);
    flag_set(ldsb, F_RE, l);
    if (roleA) {
      const int head = 4 * hkv + (j >> 2), cb128 = j & 3;
      const rsrc_t ro = mk_rsrc((const char*)p.wo + (size_t)head * 128 * 1024 + cb128 * 256, 128 * 1024 - cb128 * 256);
      unsigned vo[2];
#pragma unroll
      for (int r = 0; r < 2; ++r) vo[r] = g * 1024 + ((i16 ^ (((4 * r + g) & 7) << 1)) * 16);
      flag_wait<2, RSLP>(L, F_VI);
      flag_wait<4, RSLP>(L, F_P);
#pragma unroll 1
      for (int q = 0; q < 16; q += 2) {
        dma16<0, NT_W>(ldsb + L_K + (64 * l + 4 * q) * 256, vo[0], ro, (64 * l + 4 * q) * 1024);
        dma16<0, NT_W>(ldsb + L_K + (64 * l + 4 * q + 4) * 256, vo[1], ro, (64 * l + 4 * q + 4) * 1024);
      }
    } else {
      const int m = (8 - 2 * hi4) * hkv + 8 * hi4 + (j - 16);
      const rsrc_t rg = mk_rsrc((const char*)(l == 0 ? p.wg : p.wu) + m * 64, 512 * FF * 2 - m * 64);
      const unsigned vg = (lane >> 2) * (FF * 2) + (((lane & 3) ^ (2 * ((lane >> 4) & 1) + l)) * 16);
      const rsrc_t rd = mk_rsrc((const char*)p.wd + (size_t)m * 32 * 1024, 32 * 1024);
      flag_wait<2, RSLP>(L, F_VI);
      flag_wait<4, RSLP>(L, F_P);
#pragma unroll 1
      for (int q = 0; q < 32; ++q) {
        const unsigned qq = (q + 5 * m) & 31;
        dma16<0, NT_G>(ldsb + L_G + l * 32768 + qq * 1024, vg, rg, qq * 16 * (FF * 2));
      }
      flag_wait<4>(L, F_PV);
#pragma unroll 1
      for (int q = 0; q < 16; ++q) dma16<0, NT_W>(ldsb + L_V + (16 * l + q) * 1024, (unsigned)(lane ^ ((q & 7) << 1)) * 16, rd, (16 * l + q) * 1024);
    }
    if (roleB) vmwait<16>(); else vmwait<0>();
  } else if (w < 4) {
    const int qcb = j & 7, kq = j >> 3;
    tag = tag_after<4>(e_raw);
    if (w == 0) {
      if (lane < 4 * F_TAG) ((LAS unsigned*)(L + L_CT))[lane] = 0u;
      LGKM0();
      if (lane == 0) ((LAS unsigned*)(L + L_CT))[4 * F_TAG] = init_mark(tag);
    }
    {
      const v4u n0 = layernorm8(ld128(L, L_XN + (2 * w) * XN_S + lane * 16));
      const v4u n1 = layernorm8(ld128(L, L_XN + (2 * w + 1) * XN_S + lane * 16));
      const int t = lane >> 2, q = lane & 3;
      const unsigned base = L_XN + (2 * w) * XN_S + (32 * t + 16 * (q & 1) + 4 * (q >> 1)) * 2;
      asm volatile("" ::: "memory");
      *(LAS v2u*)(L + base) = v2u{n0.x, n0.y};
      *(LAS v2u*)(L + base + 16) = v2u{n0.z, n0.w};
      *(LAS v2u*)(L + base + XN_S) = v2u{n1.x, n1.y};
      *(LAS v2u*)(L + base + XN_S + 16) = v2u{n1.z, n1.w};
    }
    vmwait<0>();
    if (w != 0) init_wait(L, tag);
    flag_set(ldsb, F_XW, w);
    const int rr = 4 * g + (i16 >> 2), pp = i16 & 3;
    {
      unsigned ab = L_WQ + rr * 128 + (((2 * w + (pp >> 1)) ^ (((rr >> 1) & 3) << 1)) * 16) + 8 * (pp & 1);
      unsigned bb = L_XN + (i16 & 7) * XN_S + (128 * kq + 8 * g) * 2;
      unsigned qa = WS_QG + (unsigned)(((hkv * 8 + 4 * (g & 1) + (g >> 1)) * 4 + kq) * 512 + 64 * qcb + 16 * w + i16) * 4;
      PIN(ab); PIN(bb); PIN(qa);
      flag_wait<4>(L, F_XW);
      if (roleA || roleB) flag_wait<2>(L, F_RE);
      v4u a[4], bq[4];
#pragma unroll
      for (int t = 0; t < 4; ++t) { a[t] = tr_frag(L, ab + t * 4096, 2048); bq[t] = ld128(L, bb + t * 64); }
      __builtin_amdgcn_sched_barrier(0);
      f32x4 acc = {0.f, 0.f, 0.f, 0.f};
#pragma unroll
      for (int t = 0; t < 4; ++t) acc = mfma(bq[t], a[t], acc);
      gst32(ws, qa, tw(g < 2 ? acc[0] : acc[1], tag));
      gst32(ws, qa + 2 * 4 * 512 * 4, tw(g < 2 ? acc[2] : acc[3], tag));
    }
    const int hd = i16 & 3, dup = i16 >> 2;
    v4u kf[16];
    {
      const unsigned off = WS_QG + (unsigned)((hkv * 8 + b) * 4 * 512 + 128 * w + 2 * lane) * 4;
      __builtin_amdgcn_s_sleep(QD);
      v2u v[4];
#pragma unroll
      for (int k = 0; k < 4; ++k) v[k] = gld64(ws, off + k * 2048);
      flag_wait<2>(L, F_K);
#pragma unroll
      for (int T = 0; T < 4; ++T)
#pragma unroll
        for (int t = 0; t < 4; ++t) kf[4 * T + t] = ld128(L, L_K + (64 * w + 16 * T + i16) * 256 + (((4 * t + g) ^ i16) * 16));
      unsigned npass = 1;
      for (;;) {
        const unsigned bad = ((v[0].x ^ tag) | (v[0].y ^ tag) | (v[1].x ^ tag) | (v[1].y ^ tag) | (v[2].x ^ tag) | (v[2].y ^ tag) | (v[3].x ^ tag) | (v[3].y ^ tag)) & TM;
        if (__builtin_amdgcn_ballot_w64(bad != 0u) == 0ull || npass >= SPIN_CAP) break;
        asm volatile("" ::: "memory");
#pragma unroll
        for (int k = 0; k < 4; ++k) v[k] = gld64(ws, off + k * 2048);
        ++npass;
      }
      const float q0 = (tw_val(v[0].x) + tw_val(v[1].x)) + (tw_val(v[2].x) + tw_val(v[3].x));
      const float q1 = (tw_val(v[0].y) + tw_val(v[1].y)) + (tw_val(v[2].y) + tw_val(v[3].y));
      *(LAS unsigned*)(L + L_Q + w * Q_S + lane * 4) = pk(q0, q1);
    }
    flag_set(ldsb, F_Q, w);
    float sc[4];
    {
      flag_wait<4>(L, F_Q);
      v4u qf[4];
#pragma unroll
      for (int t = 0; t < 4; ++t) qf[t] = ld128(L, L_Q + hd * Q_S + (4 * t + g) * 16);
      __builtin_amdgcn_sched_barrier(0);
      f32x4 acc[4];
#pragma unroll
      for (int T = 0; T < 4; ++T) acc[T] = f32x4{0.f, 0.f, 0.f, 0.f};
#pragma unroll
      for (int t = 0; t < 4; ++t)
#pragma unroll
        for (int T = 0; T < 4; ++T) acc[T] = mfma(kf[4 * T + t], qf[t], acc[T]);
#pragma unroll
      for (int r = 0; r < 4; ++r) {
        const float a0 = acc[0][r], a1 = acc[1][r], a2 = acc[2][r], a3 = acc[3][r];
        const float a01 = (dup & 1) ? a1 : a0, a23 = (dup & 1) ? a3 : a2;
        sc[r] = rb(rb((dup & 2) ? a23 : a01) * SCALE);
      }
    }
    float inv, lse, fr[4];
    {
      float mw = fmaxf(fmaxf(sc[0], sc[1]), fmaxf(sc[2], sc[3]));
      mw = fmaxf(mw, dpp<ROR4>(mw)); mw = fmaxf(mw, dpp<ROR8>(mw));
      mw = x32_max(x16_max(mw));
      float e[4];
#pragma unroll
      for (int r = 0; r < 4; ++r) e[r] = __builtin_amdgcn_exp2f((sc[r] - mw) * LOG2E);
      *(LAS v2u*)(L + L_P + hd * P_S + (32 * (2 * w + (dup >> 1)) + 8 * g + 4 * (dup & 1)) * 2) = v2u{pk(e[0], e[1]), pk(e[2], e[3])};
      float t = (e[0] + e[1]) + (e[2] + e[3]);
      t += dpp<ROR4>(t); t += dpp<ROR8>(t);
      t = x32_sum(x16_sum(t));
      if (lane < 4) *(LAS f32x2*)(L + L_ST + (lane * 4 + w) * 8) = f32x2{mw, t};
      flag_set(ldsb, F_P, w);
      flag_wait<4>(L, F_P);
      const f32x4 s01 = *(LAS f32x4*)(L + L_ST + g * 32), s23 = *(LAS f32x4*)(L + L_ST + g * 32 + 16);
      const float mx = fmaxf(fmaxf(s01[0], s01[2]), fmaxf(s23[0], s23[2]));
      const float f0 = __builtin_amdgcn_exp2f((s01[0] - mx) * LOG2E), f1 = __builtin_amdgcn_exp2f((s01[2] - mx) * LOG2E);
      const float f2 = __builtin_amdgcn_exp2f((s23[0] - mx) * LOG2E), f3 = __builtin_amdgcn_exp2f((s23[2] - mx) * LOG2E);
      const float lsum = fmaf(f0, s01[1], f1 * s01[3]) + fmaf(f2, s23[1], f3 * s23[3]);
      inv = __builtin_amdgcn_rcpf(lsum);
      lse = fmaf(mx, LOG2E, __builtin_amdgcn_logf(lsum));
      const int r2 = rotv >> 1;
      const float ga = (r2 & 1) ? f1 : f0, gb = (r2 & 1) ? f2 : f1, gc = (r2 & 1) ? f3 : f2, gd = (r2 & 1) ? f0 : f3;
      fr[0] = (r2 & 2) ? gc : ga; fr[1] = (r2 & 2) ? gd : gb; fr[2] = (r2 & 2) ? ga : gc; fr[3] = (r2 & 2) ? gb : gd;
    }
    {
      const unsigned coblk = WS_CO + (unsigned)(hkv * 32 + j) * CO_BLK;
      if (w == 0 && i16 < 4) gst64(ws, coblk + 2048 + (g * 4 + i16) * 8, v2u{__float_as_uint(lse), tag});
      v4u pf[8];
#pragma unroll
      for (int u = 0; u < 8; ++u) pf[u] = ld128(L, L_P + dup * P_S + (32 * ((u + rotv) & 7) + 8 * g) * 2);
      const unsigned vb0 = L_V + rr * 256 + (((4 * w + (pp >> 1)) ^ ((rr & 7) << 1)) * 16) + 8 * (pp & 1);
      const unsigned vb1 = L_V + rr * 256 + (((4 * w + 2 + (pp >> 1)) ^ ((rr & 7) << 1)) * 16) + 8 * (pp & 1);
      unsigned oaddr = coblk + g * 512 + w * 128 + i16 * 8;
      PIN(oaddr); PIN(inv);
#pragma unroll
      for (int cc = 0; cc < 4; ++cc) fr[cc] *= inv;
      float o0 = 0.f, o1 = 0.f;
#pragma unroll
      for (int cc = 0; cc < 4; ++cc) {
        const unsigned ua = ((2 * cc + rotv) & 7) * 8192;
        unsigned a0 = vb0 + ua, a1 = vb1 + ua;
        PIN(a0); PIN(a1); PIN(fr[cc]);
        flag_wait<2>(L, F_V + cc);
        const v4u a00 = tr_frag(L, a0, 4096), a10 = tr_frag(L, a1, 4096);
        const v4u a01 = tr_frag(L, a0 + 8192, 4096), a11 = tr_frag(L, a1 + 8192, 4096);
        __builtin_amdgcn_sched_barrier(0);
        f32x4 c0 = mfma(pf[2 * cc], a00, f32x4{0.f, 0.f, 0.f, 0.f}), c1 = mfma(pf[2 * cc], a10, f32x4{0.f, 0.f, 0.f, 0.f});
        c0 = mfma(pf[2 * cc + 1], a01, c0); c1 = mfma(pf[2 * cc + 1], a11, c1);
        o0 = fmaf(fr[cc], c0[0], o0); o1 = fmaf(fr[cc], c1[0], o1);
        asm volatile("" : "+v"(o0), "+v"(o1));
      }
      gst64(ws, oaddr, v2u{tw(o0, tag), tw(o1, tag)});
      if (roleB) flag_set(ldsb, F_PV, w);
    }
  } else {
    const int l = w - 4;
    const rsrc_t rk = mk_rsrc((const char*)p.kc + win, 65536), rv = mk_rsrc((const char*)p.vc + win, 65536);
    unsigned vk[4], vv[2];
#pragma unroll
    for (int r = 0; r < 4; ++r) vk[r] = g * 256 + ((i16 ^ (4 * r + g)) * 16);
#pragma unroll
    for (int r = 0; r < 2; ++r) vv[r] = g * 256 + ((i16 ^ (((4 * r + g) & 7) << 1)) * 16);
    asm volatile("s_nop 4" ::: "memory");
#pragma unroll 1
    for (int a = K_FIRST; a < 8; ++a) {
      const unsigned so = ((a + rot) & 7) * 8192 + l * 4096;
      vmwait<KV_WIN - 1>(); dma16<0, NT_KV>(ldsb + L_K + so, vk[0], rk, so);
      vmwait<KV_WIN - 1>(); dma16<1024, NT_KV>(ldsb + L_K + so, vk[1], rk, so);
      vmwait<KV_WIN - 1>(); dma16<2048, NT_KV>(ldsb + L_K + so, vk[2], rk, so);
      vmwait<KV_WIN - 1>(); dma16<3072, NT_KV>(ldsb + L_K + so, vk[3], rk, so);
    }
    tag = tag_after<(KV_WIN < 31 ? KV_WIN : 31)>(e_raw);
    init_wait(L, tag);
#pragma unroll 1
    for (int a = 0; a < 8; ++a) {
      const unsigned so = ((a + rotv) & 7) * 8192 + l * 4096;
      vmwait<KV_WIN - 1>(); dma16<0, NT_KV>(ldsb + L_V + so, vv[0], rv, so);
      vmwait<KV_WIN - 1>(); dma16<1024, NT_KV>(ldsb + L_V + so, vv[1], rv, so);
      vmwait<KV_WIN - 1>(); dma16<2048, NT_KV>(ldsb + L_V + so, vv[0], rv, so);
      vmwait<KV_WIN - 1>(); dma16<3072, NT_KV>(ldsb + L_V + so, vv[1], rv, so);
      if (a == 2) { vmwait<(KV_WIN < 12 ? KV_WIN : 12)>(); flag_set(ldsb, F_K, l); }
      if (roleA && a == RWA_START - 1) flag_set(ldsb, F_VI, l);
    }
    if (KV_WIN > 24) vmwait<24>();
    flag_set(ldsb, F_V + 0, l);
    if (KV_WIN > 16) vmwait<16>();
    flag_set(ldsb, F_V + 1, l);
    vmwait<8>(); flag_set(ldsb, F_V + 2, l);
    vmwait<0>(); flag_set(ldsb, F_V + 3, l);
    if (!roleA) flag_set(ldsb, F_VI, l);
    if (roleA) {
      const unsigned os = WS_CO + (unsigned)(hkv * 32 + 4 * (4 * l + g) + (i16 & 3)) * CO_BLK + 2048 + (j >> 2) * 32;
      v2u st = gld64(ws, os);
#pragma unroll 1
      for (unsigned n = 1; __builtin_amdgcn_ballot_w64(st.y != tag) != 0ull && n < SPIN_CAP; ++n) {
        __builtin_amdgcn_s_sleep(2);
        asm volatile("" ::: "memory");
        st = gld64(ws, os);
      }
      const float Ls = __uint_as_float(st.x);
      float Lm = fmaxf(Ls, dpp<XOR1>(Ls)); Lm = fmaxf(Lm, dpp<XOR2>(Lm));
      const float e = __builtin_amdgcn_exp2f(Ls - Lm);
      float es = e + dpp<XOR1>(e); es += dpp<XOR2>(es);
      flag_wait<4>(L, F_P);
      if (i16 < 4) *(LAS float*)(L + L_W + (4 * l + g) * 16 + i16 * 4) = e * __builtin_amdgcn_rcpf(es);
    }
  }

  const int rr = 4 * g + (i16 >> 2), pp = i16 & 3;
  if (roleA) {
    const int hl = j >> 2, cb128 = j & 3, head = 4 * hkv + hl;
    const unsigned pb = WS_CO + (unsigned)(hkv * 32 + 4 * w) * CO_BLK;
    const unsigned o0 = pb + (lane >> 5) * CO_BLK + hl * 512 + (lane & 31) * 16;
    const int ch = lane & 31;
    unsigned owr = L_O + w * O_S + (32 * (ch >> 3) + 8 * ((ch & 7) >> 1) + 2 * (ch & 1)) * 2;
    unsigned rpaddr = WS_RP + (unsigned)((head * 8 + 4 * (g & 1) + (g >> 1)) * 512 + cb128 * 128 + 16 * w + i16) * 4;
    unsigned obase = L_O + (i16 & 7) * O_S + 8 * g * 2;
    unsigned ab = L_K + rr * 256 + (((2 * w + (pp >> 1)) ^ ((rr & 7) << 1)) * 16) + 8 * (pp & 1);
    PIN(owr); PIN(rpaddr); PIN(obase); PIN(ab);
    wg_barrier();
    v4u af[4];
    const f32x4 w4 = *(LAS f32x4*)(L + L_W + w * 16);
    const float wa = lane < 32 ? w4[0] : w4[1], wb = lane < 32 ? w4[2] : w4[3];
    {
      if (CO_DLY) __builtin_amdgcn_s_sleep(CO_DLY);
      v4u v0 = gld128(ws, o0), v1 = gld128(ws, o0 + 2 * CO_BLK);
#pragma unroll
      for (int t = 0; t < 4; ++t) af[t] = tr_frag(L, ab + t * 8192, 4096);
      unsigned npass = 1;
      while (__builtin_amdgcn_ballot_w64((((v0.x ^ tag) | (v0.y ^ tag) | (v0.z ^ tag) | (v0.w ^ tag) | (v1.x ^ tag) | (v1.y ^ tag) | (v1.z ^ tag) | (v1.w ^ tag)) & TM) != 0u) != 0ull &&
             npass < SPIN_CAP) {
        asm volatile("" ::: "memory");
        v0 = gld128(ws, o0); v1 = gld128(ws, o0 + 2 * CO_BLK);
        ++npass;
      }
      const float a0 = x32_sum(fmaf(wa, tw_val(v0.x), wb * tw_val(v1.x))), a1 = x32_sum(fmaf(wa, tw_val(v0.z), wb * tw_val(v1.z)));
      const float a2 = x32_sum(fmaf(wa, tw_val(v0.y), wb * tw_val(v1.y))), a3 = x32_sum(fmaf(wa, tw_val(v0.w), wb * tw_val(v1.w)));
      if (lane < 32) { *(LAS unsigned*)(L + owr) = pk(a0, a1); *(LAS unsigned*)(L + owr + 8) = pk(a2, a3); }
    }
    wg_barrier();
    {
      v4u bo[4];
#pragma unroll
      for (int t = 0; t < 4; ++t) bo[t] = ld128(L, obase + t * 64);
      __builtin_amdgcn_sched_barrier(0);
      f32x4 acc = {0.f, 0.f, 0.f, 0.f};
#pragma unroll
      for (int t = 0; t < 4; ++t) acc = mfma(bo[t], af[t], acc);
      gst32(ws, rpaddr, tw(g < 2 ? acc[0] : acc[1], tag));
      gst32(ws, rpaddr + 2 * 512 * 4, tw(g < 2 ? acc[2] : acc[3], tag));
    }
    if (w == 0) {
      const int br = j & 7, cb32 = (j >> 3) * 8 + hkv, grp = lane >> 3, q8 = lane & 7;
      const v2u xr = ((const v2u*)p.x)[br * 128 + cb32 * 8 + q8];
      const unsigned off = WS_RP + (unsigned)((grp * 8 + br) * 512 + 32 * cb32 + 4 * q8) * 4;
      const unsigned hoff = WS_HG + (unsigned)(br * 256 + 16 * cb32 + 2 * q8) * 8;
      if (R_DLY) __builtin_amdgcn_s_sleep(R_DLY);
      v4u v[4];
      unsigned npass = 0;
      for (;;) {
        gpass<4>(ws, off, 8u * 8 * 512 * 4, v);
        ++npass;
        unsigned bad = 0;
#pragma unroll
        for (int k = 0; k < 4; ++k) bad |= (v[k].x ^ tag) | (v[k].y ^ tag) | (v[k].z ^ tag) | (v[k].w ^ tag);
        if (__builtin_amdgcn_ballot_w64((bad & TM) != 0u) == 0ull || npass >= SPIN_CAP) break;
        if (R_SLP) __builtin_amdgcn_s_sleep(R_SLP);
      }
      float s0 = (tw_val(v[0].x) + tw_val(v[1].x)) + (tw_val(v[2].x) + tw_val(v[3].x)), s1 = (tw_val(v[0].y) + tw_val(v[1].y)) + (tw_val(v[2].y) + tw_val(v[3].y));
      float s2 = (tw_val(v[0].z) + tw_val(v[1].z)) + (tw_val(v[2].z) + tw_val(v[3].z)), s3 = (tw_val(v[0].w) + tw_val(v[1].w)) + (tw_val(v[2].w) + tw_val(v[3].w));
      s0 += dpp<ROR8>(s0); s1 += dpp<ROR8>(s1); s2 += dpp<ROR8>(s2); s3 += dpp<ROR8>(s3);
      s0 = x32_sum(x16_sum(s0)); s1 = x32_sum(x16_sum(s1)); s2 = x32_sum(x16_sum(s2)); s3 = x32_sum(x16_sum(s3));
      const unsigned h01 = pk(lo16(xr.x) + rb(s0), hi16(xr.x) + rb(s1)), h23 = pk(lo16(xr.y) + rb(s2), hi16(xr.y) + rb(s3));
      if (grp < HG_N) gst128(ws, hoff + grp * HG_COPY, v4u{h01, tag, h23, tag});
      if (c == 0 && lane == 0) gst32(ws, WS_E, tag);
    } else if (w == 1) {
      const int a = hkv * 16 + j, grp = lane >> 3, bb8 = lane & 7;
      const unsigned off = WS_R2 + (unsigned)grp * 16384 + (a >> 3) * 1024 + (a & 3) * 256 + ((a >> 2) & 1) * 128 + bb8 * 16;
      __builtin_amdgcn_s_sleep(32);
      v4u hv[1];
      gather<1, 8>(ws, WS_HG + (hkv % HG_N) * HG_COPY + (unsigned)(bb8 * 256 + 2 * a) * 8, 0, tag, hv);
      v4u v[7];
      unsigned npass = 0;
      for (;;) {
        gpass<7>(ws, off, 8u * 16384, v);
        ++npass;
        unsigned bad = 0;
#pragma unroll
        for (int k = 0; k < 7; ++k) bad |= (v[k].x ^ tag) | (v[k].y ^ tag) | (v[k].z ^ tag) | (v[k].w ^ tag);
        if (__builtin_amdgcn_ballot_w64((bad & TM) != 0u) == 0ull || npass >= SPIN_CAP) break;
        if (R2_SLP) __builtin_amdgcn_s_sleep(R2_SLP);
      }
      float s0 = tw_val(v[0].x), s1 = tw_val(v[0].y), s2 = tw_val(v[0].z), s3 = tw_val(v[0].w);
#pragma unroll
      for (int k = 1; k < 7; ++k) { s0 += tw_val(v[k].x); s1 += tw_val(v[k].y); s2 += tw_val(v[k].z); s3 += tw_val(v[k].w); }
      s0 += dpp<ROR8>(s0); s1 += dpp<ROR8>(s1); s2 += dpp<ROR8>(s2); s3 += dpp<ROR8>(s3);
      s0 = x32_sum(x16_sum(s0)); s1 = x32_sum(x16_sum(s1)); s2 = x32_sum(x16_sum(s2)); s3 = x32_sum(x16_sum(s3));
      const v2u res = {pk(lo16(hv[0].x) + rb(s0), hi16(hv[0].x) + rb(s1)), pk(lo16(hv[0].z) + rb(s2), hi16(hv[0].z) + rb(s3))};
      if (lane < 8) ((v2u*)p.out)[bb8 * 128 + a] = res;
    }
  } else if (roleB) {
    const int m = (8 - 2 * hi4) * hkv + 8 * hi4 + (j - 16);
    unsigned r2addr = WS_R2 + (unsigned)m * 16384 + 2 * w * 1024 + lane * 16;
    unsigned bb = L_XN + (i16 & 7) * XN_S + 8 * g * 2;
    unsigned hnw = L_XN + w * XN_S + (32 * (lane >> 3) + 8 * (lane & 3) + 4 * ((lane >> 2) & 1)) * 2;
    unsigned hhr = L_HH + (i16 & 7) * HH_S + g * 16;
    unsigned hhw = L_HH + i16 * HH_S + (8 * ((2 * w + g) & 3) + 4 * ((2 * w + g) >> 2)) * 2;
    const unsigned hoff = WS_HG + (hkv % HG_N) * HG_COPY + (unsigned)(w * 256 + 2 * lane) * 8;
    PIN(r2addr); PIN(bb); PIN(hnw); PIN(hhr); PIN(hhw);
    wg_barrier();
    v4u df[4], gf[16];
    if (w < 4) {
      const int mat = pp >> 1;
      const unsigned gb = L_G + mat * 32768 + rr * 64 + ((w ^ (2 * ((rr >> 2) & 1) + mat)) * 16) + 8 * (pp & 1);
#pragma unroll
      for (int t = 0; t < 16; ++t) gf[t] = tr_frag(L, gb + t * 2048, 1024);
    }
    {
      v4u hv[2];
      gather<2, H_SLP>(ws, hoff, 1024, tag, hv);
      const v4u hn = layernorm8(v4u{hv[0].x, hv[0].z, hv[1].x, hv[1].z});
      *(LAS v2u*)(L + hnw) = v2u{hn.x, hn.y};
      *(LAS v2u*)(L + hnw + 512) = v2u{hn.z, hn.w};
    }
    if (w >= 6) vmwait<0>();
    wg_barrier();
    if (w < 4) {
      v4u hb[16];
#pragma unroll
      for (int t = 0; t < 16; ++t) hb[t] = ld128(L, bb + t * 64);
      __builtin_amdgcn_sched_barrier(0);
      f32x4 acc = {0.f, 0.f, 0.f, 0.f}, acc2 = {0.f, 0.f, 0.f, 0.f};
#pragma unroll
      for (int t = 0; t < 16; t += 2) { acc = mfma(gf[t], hb[t], acc); acc2 = mfma(gf[t + 1], hb[t + 1], acc2); }
#pragma unroll
      for (int x = 0; x < 4; ++x)
        df[x] = tr_frag(L, L_V + rr * 1024 + (((2 * (4 * w + x) + (pp >> 1)) ^ ((rr & 7) << 1)) * 16) + 8 * (pp & 1), 16 * 1024);
      float hh[4];
#pragma unroll
      for (int r = 0; r < 4; ++r) {
        const float v = rb(acc[r] + acc2[r]);
        auto sw = __builtin_amdgcn_permlane32_swap(__float_as_uint(v), __float_as_uint(v), false, false);
        const float gt = __uint_as_float(sw[0]), up = __uint_as_float(sw[1]);
        const float sg = rb(__builtin_amdgcn_rcpf(1.0f + __builtin_amdgcn_exp2f(-gt * LOG2E)));
        hh[r] = sg * up;
      }
      if (lane < 32 && i16 < 8) *(LAS v2u*)(L + hhw) = v2u{pk(hh[0], hh[1]), pk(hh[2], hh[3])};
    } else {
#pragma unroll
      for (int x = 0; x < 4; ++x)
        df[x] = tr_frag(L, L_V + rr * 1024 + (((2 * (4 * w + x) + (pp >> 1)) ^ ((rr & 7) << 1)) * 16) + 8 * (pp & 1), 16 * 1024);
    }
    wg_barrier();
    {
      const v4u bf = ld128(L, hhr);
      const int half = i16 >> 3;
      f32x4 acc[4];
#pragma unroll
      for (int x = 0; x < 4; ++x) acc[x] = mfma(df[x], bf, f32x4{0.f, 0.f, 0.f, 0.f});
#pragma unroll
      for (int y = 0; y < 2; ++y) {
        const f32x4 e = acc[2 * y], d = acc[2 * y + 1];
        const float o0 = half ? d[0] : e[0], o1 = half ? d[1] : e[1], o2 = half ? d[2] : e[2], o3 = half ? d[3] : e[3];
        gst128(ws, r2addr + y * 1024, v4u{tw(o0, tag), tw(o1, tag), tw(o2, tag), tw(o3, tag)});
      }
    }
  }
}
}

extern "C" void kernel_launch(void* const* d_in, const int* in_sizes, int n_in, void* d_out, int out_size,
                              void* d_ws, size_t ws_size, hipStream_t stream) {
  fk::Params p;
  p.x = d_in[0]; p.kc = d_in[1]; p.vc = d_in[2]; p.wq = d_in[3]; p.wo = d_in[4];
  p.wg = d_in[6]; p.wu = d_in[7]; p.wd = d_in[8]; p.out = d_out; p.ws = d_ws;
  hipLaunchKernelGGL(fk::fused_decode, dim3(256), dim3(512), 0, stream, p);
}
```
